```python
import torch

def _softmax(x, axis=-1):
    return torch.softmax(x, dim=axis)
def _gelu(x):
    return torch.nn.functional.gelu(x, approximate="tanh")
def _silu(x):
    return torch.nn.functional.silu(x)
def _sigmoid(x):
    return torch.sigmoid(x)
def _layernorm(x, axis=-1, eps=1e-5):
    m = x.mean(dim=axis, keepdim=True)
    v = x.var(dim=axis, keepdim=True, unbiased=False)
    return (x - m) / torch.sqrt(v + eps)
def _scatter_add(tgt, idx, src, axis=0):
    return tgt.clone().index_add_(axis, idx, src)
def _segment_sum(src, seg, n):
    out = torch.zeros((n,) + src.shape[1:], dtype=src.dtype)
    return out.index_add_(0, seg, src)
def _pad(x, pw):
    flat = tuple(v for pair in reversed(pw) for v in pair)
    return torch.nn.functional.pad(x, flat)

kernel_name = 'interleave_layers__linear_kernel_hedgehog__mixer_s_mlp__prompt'

def setup_inputs():

    _w = (lambda a: (lambda t: t.to(torch.bfloat16) if t.is_floating_point() else t)(torch.as_tensor(a)))
    def _A_setup():
        B, S, D, H = 1, 2048, 640, 32
        Dh = D // H
        return {
            "x": _w((lambda *s: torch.randn(*s, dtype=torch.bfloat16) * 0.05)(B, S, D)),
            "wqkv": _w((lambda *s: torch.randn(*s, dtype=torch.bfloat16) * 0.4)(D, 3 * D)),
            "w_phi": _w((lambda *s: torch.randn(*s, dtype=torch.bfloat16) * 0.4)(H, Dh, Dh) * (Dh ** -0.5)),
            "wo": _w((lambda *s: torch.randn(*s, dtype=torch.bfloat16) * 0.05)(D, D)),
        }
    _A_in = _A_setup()

    def _B_setup():
        B, S, D, F = 1, 2048, 640, 1536
        return {
            "x":  _w((lambda *s: torch.randn(*s, dtype=torch.bfloat16) * 0.05)(B, S, D)),
            "w1": _w((lambda *s: torch.randn(*s, dtype=torch.bfloat16) * 3.2)(D, D) * (D ** -0.5)),
            "w2": _w((lambda *s: torch.randn(*s, dtype=torch.bfloat16) * 3.2)(D, D) * (D ** -0.5)),
            "w3": _w((lambda *s: torch.randn(*s, dtype=torch.bfloat16) * 3.2)(D, F) * (D ** -0.5)),
            "w4": _w((lambda *s: torch.randn(*s, dtype=torch.bfloat16) * 3.2)(F, D) * (F ** -0.5)),
        }
    _B_in = _B_setup()

    return {
        "A_x": _A_in["x"],
        "A_wqkv": _A_in["wqkv"],
        "A_w_phi": _A_in["w_phi"],
        "A_wo": _A_in["wo"],
        "B_x": _B_in["x"],
        "B_w1": _B_in["w1"],
        "B_w2": _B_in["w2"],
        "B_w3": _B_in["w3"],
        "B_w4": _B_in["w4"],
    }

def reference(A_x, A_wqkv, A_w_phi, A_wo, B_x, B_w1, B_w2, B_w3, B_w4):
    def _A_ref(x, wqkv, w_phi, wo):
        B, S, D = x.shape
        H = 32; Dh = D // H
        qkv = (lambda x, *d: x.permute(*d))((x @ wqkv).reshape(B, S, 3, H, Dh), 2, 0, 3, 1, 4)
        q, k, v = qkv[0], qkv[1], qkv[2]
        pq = torch.einsum("bhsd,hdf->bhsf", q, w_phi)
        pk = torch.einsum("bhsd,hdf->bhsf", k, w_phi)
        phi_q = torch.concatenate([_softmax(pq, axis=-1), _softmax(-pq, axis=-1)], axis=-1)
        phi_k = torch.concatenate([_softmax(pk, axis=-1), _softmax(-pk, axis=-1)], axis=-1)
        kv = torch.einsum("bhsf,bhsd->bhfd", phi_k, v)
        z  = torch.einsum("bhsf->bhf", phi_k)
        num = torch.einsum("bhqf,bhfd->bhqd", phi_q, kv)
        den = torch.einsum("bhqf,bhf->bhq", phi_q, z)[..., None] + 1e-6
        o = num / den
        return (lambda x, *d: x.permute(*d))(o, 0, 2, 1, 3).reshape(B, S, D) @ wo

    def _B_ref(x, w1, w2, w3, w4):
        B, S, D = x.shape
        h = _gelu(_layernorm(x, axis=-1) @ w1)
        c = D // 4
        g0 = _pad(h[:, :, 0*c:1*c], ((0, 0), (1, 0), (0, 0)))[:, :S, :]
        g1 = _pad(h[:, :, 1*c:2*c], ((0, 0), (0, 1), (0, 0)))[:, 1:, :]
        g2 = _pad(h[:, :, 2*c:3*c], ((0, 0), (2, 0), (0, 0)))[:, :S, :]
        g3 = _pad(h[:, :, 3*c:   ], ((0, 0), (0, 2), (0, 0)))[:, 2:, :]
        hs = torch.concatenate([g0, g1, g2, g3], axis=-1) @ w2
        x1 = x + hs
        h2 = _gelu(_layernorm(x1, axis=-1) @ w3) @ w4
        return x1 + h2

    x = A_x
    x = x + _A_ref(_layernorm(x, axis=-1), A_wqkv, A_w_phi, A_wo)
    x = x + _B_ref(_layernorm(x, axis=-1), B_w1, B_w2, B_w3, B_w4)
    x = x + _A_ref(_layernorm(x, axis=-1), A_wqkv, A_w_phi, A_wo)
    x = x + _B_ref(_layernorm(x, axis=-1), B_w1, B_w2, B_w3, B_w4)
    return x
```

```cpp
#include <hip/hip_runtime.h>
#include <stdint.h>

namespace hk {

#define DI __device__ __forceinline__
#define GAS __attribute__((address_space(1)))
#define LAS __attribute__((address_space(3)))
typedef unsigned v4u __attribute__((ext_vector_type(4)));
typedef unsigned v2u __attribute__((ext_vector_type(2)));
typedef float f4 __attribute__((ext_vector_type(4)));
typedef float f2 __attribute__((ext_vector_type(2)));
typedef short s8 __attribute__((ext_vector_type(8)));
typedef short s4 __attribute__((ext_vector_type(4)));
typedef __bf16 bf2 __attribute__((ext_vector_type(2)));
typedef __amdgpu_buffer_rsrc_t rsrc_t;
typedef unsigned long long u64;

constexpr int SEQ = 2048, DM = 640, FF = 1536;
constexpr float LN_EPS = 1e-5f;
constexpr float LOG2E = 1.4426950408889634f;

constexpr int OFF_EP = 0;
constexpr int OFF_ABORT = 256;
constexpr int OFF_FB = 4096;
constexpr int OFF_FLAG = 8192;
constexpr int FG_A1 = 0;
constexpr int FG_A2 = 32;
constexpr int FG_L = 64;
constexpr int FG_KV = 0, FG_O = 32, FG_X2 = 64, FG_H = 96, FG_X1 = 128, FG_H2 = 160, FG_X3 = 192, FG_PER_L = 224;
constexpr int OFF_WQ = 1 << 18;
constexpr int SZ_WQ = 32 * 4 * 80 * 256;
constexpr int OFF_WO = OFF_WQ + SZ_WQ;
constexpr int SZ_W6 = 40 * 80 * 256;
constexpr int OFF_W1 = OFF_WO + SZ_W6;
constexpr int OFF_W2 = OFF_W1 + SZ_W6;
constexpr int OFF_W3 = OFF_W2 + SZ_W6;
constexpr int SZ_W3 = 96 * 80 * 256;
constexpr int OFF_W4 = OFF_W3 + SZ_W3;
constexpr int SZ_W4 = 40 * 192 * 256;
constexpr int OFF_CSQ = OFF_W4 + SZ_W4;
constexpr int OFF_CS1 = OFF_CSQ + 32 * 8 * 64 * 4;
constexpr int OFF_CS3 = OFF_CS1 + 20 * 640 * 4;
constexpr int OFF_ST0 = OFF_CS3 + 20 * 1536 * 4;
constexpr int TILE6 = 80 * 256;
constexpr int TILEF = 192 * 256;
constexpr int SZ_A6 = 128 * TILE6, SZ_AF = 128 * TILEF;
constexpr int SZ_ST = 128 * 1024;
constexpr int SZ_KVP = 32 * 8 * 6144;
constexpr int OFF_ACT = OFF_ST0 + SEQ * 8;
constexpr int A_O = 0, A_X2 = A_O + SZ_A6, A_H = A_X2 + SZ_A6, A_X1 = A_H + SZ_A6, A_X3 = A_X1 + SZ_A6, A_H2 = A_X3 + SZ_A6,
              A_S2 = A_H2 + SZ_AF, A_S1 = A_S2 + SZ_ST, A_S3 = A_S1 + SZ_ST, A_KVP = A_S3 + SZ_ST, SZ_LAYER = A_KVP + SZ_KVP;
constexpr int OFF_PROF = OFF_ACT + 2 * SZ_LAYER;
constexpr int WS_USED = OFF_PROF + 256 * 64 * 8;
constexpr unsigned OOB = 0x80000000u;

constexpr int L_BIG = 0;
constexpr int L_SROW = 81920;
constexpr int SROW = 168;
constexpr int L_KVW = 0;
constexpr int L_KV = 32768;
constexpr int L_SMALL = 126976;
constexpr int L_CSQ = L_SMALL;
constexpr int L_CS1 = L_CSQ + 256;
constexpr int L_CS3 = L_CS1 + 320;
constexpr int LDS_BYTES = 131072;

constexpr int AUX_SC1 = 16;
constexpr unsigned SPIN_CAP = 1u << 17;

struct Params {
  const unsigned short* x;
  const unsigned short* wqkv;
  const unsigned short* wphi;
  const unsigned short* wo;
  const unsigned short* w1;
  const unsigned short* w2;
  const unsigned short* w3;
  const unsigned short* w4;
  unsigned short* out;
  char* ws;
};

DI f4 mfma(v4u a, v4u b, f4 c) {
  return __builtin_amdgcn_mfma_f32_16x16x32_bf16(__builtin_bit_cast(s8, a), __builtin_bit_cast(s8, b), c, 0, 0, 0);
}
DI unsigned pk2(float a, float b) {
  bf2 v = {(__bf16)a, (__bf16)b};
  return __builtin_bit_cast(unsigned, v);
}
DI float blo(unsigned u) { return __builtin_bit_cast(float, u << 16); }
DI float bhi(unsigned u) { return __builtin_bit_cast(float, u & 0xffff0000u); }
DI v4u ld16(rsrc_t r, unsigned voff, int soff) { return __builtin_amdgcn_raw_buffer_load_b128(r, voff, soff, AUX_SC1); }
DI v2u ld8(rsrc_t r, unsigned voff, int soff) { return __builtin_amdgcn_raw_buffer_load_b64(r, voff, soff, AUX_SC1); }
DI unsigned ld4(rsrc_t r, unsigned voff, int soff) { return __builtin_amdgcn_raw_buffer_load_b32(r, voff, soff, AUX_SC1); }
DI v4u ld16p(rsrc_t r, unsigned voff, int soff) { return __builtin_amdgcn_raw_buffer_load_b128(r, voff, soff, 0); }
DI v2u ld8p(rsrc_t r, unsigned voff, int soff) { return __builtin_amdgcn_raw_buffer_load_b64(r, voff, soff, 0); }
DI void st16(rsrc_t r, unsigned voff, int soff, v4u v) { __builtin_amdgcn_raw_buffer_store_b128(v, r, voff + (unsigned)soff, 0, AUX_SC1); }
DI void st8(rsrc_t r, unsigned voff, int soff, v2u v) { __builtin_amdgcn_raw_buffer_store_b64(v, r, voff, soff, AUX_SC1); }
DI void st4(rsrc_t r, unsigned voff, int soff, unsigned v) { __builtin_amdgcn_raw_buffer_store_b32(v, r, voff, soff, AUX_SC1); }
DI float asf(unsigned u) { return __builtin_bit_cast(float, u); }
DI unsigned asu(float f) { return __builtin_bit_cast(unsigned, f); }
DI f4 asf4(v4u u) { return __builtin_bit_cast(f4, u); }
DI v4u asu4(f4 f) { return __builtin_bit_cast(v4u, f); }

DI float xsum(float x) {
  auto a = __builtin_amdgcn_permlane16_swap(asu(x), asu(x), false, false);
  x = asf(a[0]) + asf(a[1]);
  auto b = __builtin_amdgcn_permlane32_swap(asu(x), asu(x), false, false);
  return asf(b[0]) + asf(b[1]);
}
template <bool MAX> DI float rop(float a, float b) { return MAX ? __builtin_fmaxf(a, b) : a + b; }
template <bool MAX> DI void xred2(float& X, float& Y) {
  auto a = __builtin_amdgcn_permlane32_swap(asu(X), asu(Y), false, false);
  float c = rop<MAX>(asf(a[0]), asf(a[1]));
  auto d = __builtin_amdgcn_permlane16_swap(asu(c), asu(c), false, false);
  float e = rop<MAX>(asf(d[0]), asf(d[1]));
  auto f = __builtin_amdgcn_permlane32_swap(asu(e), asu(e), false, false);
  X = asf(f[0]);
  Y = asf(f[1]);
}
DI float gelu(float x) {
  const float c0 = -2.302208198f, c1 = -0.1029432397f;
  float t = __builtin_fmaf(x * x, c1, c0);
  float e = __builtin_amdgcn_exp2f(x * t);
  return x * __builtin_amdgcn_rcpf(1.0f + e);
}

#define DRAIN() asm volatile("s_waitcnt vmcnt(0)" ::: "memory")
#define BAR() __syncthreads()

struct Ctx {
  rsrc_t ws;
  u64 tag;
  char* wsp;
  int lane, w, tok, g;
};

DI GAS u64* flag_ptr(const Ctx& c, int group, int idx) { return (GAS u64*)(c.wsp + OFF_FLAG + group * 256 + idx * 8); }
DI void publish(const Ctx& c, int group, int idx) {
  DRAIN();
  BAR();
  if (threadIdx.x == 0) __hip_atomic_store(flag_ptr(c, group, idx), c.tag, __ATOMIC_RELAXED, __HIP_MEMORY_SCOPE_AGENT);
}
DI void wait_flags(const Ctx& c, int byte_off, bool active) {
  GAS u64* p = (GAS u64*)(c.wsp + byte_off);
  GAS u64* ab = (GAS u64*)(c.wsp + OFF_ABORT);
  for (unsigned spins = 0;;) {
    u64 v = c.tag;
    if (active) v = __hip_atomic_load(p, __ATOMIC_RELAXED, __HIP_MEMORY_SCOPE_AGENT);
    if (__all(v == c.tag)) break;
    ++spins;
    if ((spins & 63u) == 0u) {
      u64 a = __hip_atomic_load(ab, __ATOMIC_RELAXED, __HIP_MEMORY_SCOPE_AGENT);
      if (__any(a == c.tag)) break;
      if (spins > SPIN_CAP) {
        __hip_atomic_store(ab, c.tag, __ATOMIC_RELAXED, __HIP_MEMORY_SCOPE_AGENT);
        break;
      }
    }
  }
  __builtin_amdgcn_fence(__ATOMIC_ACQUIRE, "wavefront");
  asm volatile("" ::: "memory");
}
DI void wait_group(const Ctx& c, int group, int first, int n) {
  wait_flags(c, OFF_FLAG + group * 256 + (first + (c.lane < n ? c.lane : 0)) * 8, c.lane < n);
}

#define STAMP(i) do {} while (0)

DI v4u gather8(const unsigned short* p, int stride) {
  unsigned short e[8];
#pragma unroll
  for (int j = 0; j < 8; ++j) e[j] = p[(size_t)j * stride];
  v4u v;
  v.x = e[0] | ((unsigned)e[1] << 16);
  v.y = e[2] | ((unsigned)e[3] << 16);
  v.z = e[4] | ((unsigned)e[5] << 16);
  v.w = e[6] | ((unsigned)e[7] << 16);
  return v;
}
DI float sum8(v4u v) { return ((blo(v.x) + bhi(v.x)) + (blo(v.y) + bhi(v.y))) + ((blo(v.z) + bhi(v.z)) + (blo(v.w) + bhi(v.w))); }

DI void stage_a(const Params& P, const Ctx& c, int G, int h) {
  const int nt = c.w, r = c.tok, gp = r >> 2, i = r & 3, v = 4 * nt + i;
  const int sec = v / 5, d = 5 * gp + (v - 5 * sec);
  const bool valid = v < 15;
  const int col = valid ? sec * 640 + 20 * h + d : 0;
  v4u f[3];
#pragma unroll
  for (int q = 0; q < 3; ++q) {
    const int kg = 10 * G + 4 * q + ((q == 2) ? (c.g & 1) : c.g);
    f[q] = gather8(P.wqkv + (size_t)(8 * kg) * 1920 + col, 1920);
  }
  const int tk = 256 * G + 8 * h + 2 * c.w + (c.lane >> 5);
  const unsigned* xr = (const unsigned*)(P.x + (size_t)tk * DM) + (c.lane & 31);
  unsigned xv[10];
#pragma unroll
  for (int j = 0; j < 10; ++j) xv[j] = xr[32 * j];
  float cs = 0.f;
#pragma unroll
  for (int q = 0; q < 3; ++q) {
    if (!valid) f[q] = v4u{0u, 0u, 0u, 0u};
    const bool on = (q < 2) || (c.g < 2);
    const int kg = 10 * G + 4 * q + c.g;
    st16(c.ws, on ? (unsigned)(((h * 4 + nt) * 80 + kg) * 256 + r * 16) : OOB, OFF_WQ, f[q]);
    cs += on ? sum8(f[q]) : 0.f;
  }
  cs = xsum(cs);
  st4(c.ws, c.g == 0 ? (unsigned)(((h * 8 + G) * 64 + nt * 16 + r) * 4) : OOB, OFF_CSQ, asu(cs));
  float s = 0.f, q2 = 0.f;
#pragma unroll
  for (int j = 0; j < 10; ++j) {
    const float a = blo(xv[j]), b = bhi(xv[j]);
    s += a + b;
    q2 += a * a + b * b;
  }
#pragma unroll
  for (int o = 1; o < 32; o <<= 1) {
    s += __shfl_xor(s, o);
    q2 += __shfl_xor(q2, o);
  }
  st8(c.ws, (c.lane & 31) == 0 ? (unsigned)(tk * 8) : OOB, OFF_ST0, v2u{asu(s), asu(q2)});
}

DI void stage_b(const Params& P, const Ctx& c, int widx) {
  const int t0 = widx * 6240 / 256, t1 = (widx + 1) * 6240 / 256;
  constexpr int NT = 7;
  v4u f[NT];
  const int r = c.tok, gp = r >> 2, i = r & 3;
#pragma unroll
  for (int u = 0; u < NT; ++u) {
    int t = t0 + c.w + 4 * u;
    if (t >= t1) t = t0;
    const unsigned short* W;
    int N, kq, ntg, c0, nt;
    if (t < 2400) {
      const int m = t / 800, tt = t - 800 * m;
      W = m == 0 ? P.wo : (m == 1 ? P.w1 : P.w2);
      N = 640; kq = tt / 40; ntg = tt - 40 * kq;
      const int sl = ntg / 5; nt = ntg - 5 * sl; c0 = 80 * sl;
    } else if (t < 4320) {
      const int tt = t - 2400;
      W = P.w3; N = 1536; kq = tt / 96; ntg = tt - 96 * kq;
      const int sl = ntg / 6; nt = ntg - 6 * sl; c0 = 96 * sl;
      if (nt >= 4) { c0 += 64; nt -= 4; }
    } else {
      const int tt = t - 4320;
      W = P.w4; N = 640; kq = tt / 40; ntg = tt - 40 * kq;
      const int sl = ntg / 5; nt = ntg - 5 * sl; c0 = 80 * sl;
    }
    const int col = nt < 4 ? c0 + 32 * (nt >> 1) + 8 * gp + 4 * (nt & 1) + i : c0 + 64 + r;
    f[u] = gather8(W + (size_t)(32 * kq + 8 * c.g) * N + col, N);
  }
#pragma unroll
  for (int u = 0; u < NT; ++u) {
    const int t = t0 + c.w + 4 * u;
    const bool on = t < t1;
    int base, KG, kq, ntg, csoff = -1;
    if (t < 2400) {
      const int m = t / 800, tt = t - 800 * m;
      base = OFF_WO + m * SZ_W6; KG = 80; kq = tt / 40; ntg = tt - 40 * kq;
      if (m == 1) csoff = OFF_CS1 + kq * 640 * 4;
    } else if (t < 4320) {
      const int tt = t - 2400;
      base = OFF_W3; KG = 80; kq = tt / 96; ntg = tt - 96 * kq;
      csoff = OFF_CS3 + kq * 1536 * 4;
    } else {
      const int tt = t - 4320;
      base = OFF_W4; KG = 192; kq = tt / 40; ntg = tt - 40 * kq;
    }
    st16(c.ws, on ? (unsigned)((ntg * KG + 4 * kq + c.g) * 256 + r * 16) : OOB, base, f[u]);
    const float cs = xsum(sum8(f[u]));
    st4(c.ws, (on && csoff >= 0 && c.g == 0) ? (unsigned)((ntg * 16 + r) * 4) : OOB, csoff >= 0 ? csoff : 0, asu(cs));
  }
}

template <int KS> DI void load_w5(const Ctx& c, int wbase, int j, v4u (&wf)[5][KS]) {
#pragma unroll
  for (int nt = 0; nt < 5; ++nt)
#pragma unroll
    for (int u = 0; u < KS; ++u) wf[nt][u] = ld16(c.ws, c.lane * 16, wbase + ((5 * j + nt) * (16 * KS) + 4 * (KS * c.w + u)) * 256);
}

#define SB() __builtin_amdgcn_sched_barrier(0)
template <int KS, int LA, int NPRE, int STB, class F>
DI void gemm5(const Ctx& c0, LAS char* lds, int xoff, int tile, int p, int shift, const v4u (&wf)[5][KS], f4 (&out)[5], F pre) {
  const Ctx& c = c0;
  v4u xf[LA][KS];
  unsigned voff[4];
#pragma unroll
  for (int pos = 0; pos < 4; ++pos) {
    const int tt = (c.w + 1 + pos) & 3;
    const int t = 16 * (4 * p + tt) + c.tok + shift;
    voff[pos] = (t >= 0 && t < SEQ) ? (unsigned)((t >> 4) * tile + c.g * 256 + (t & 15) * 16) : OOB;
  }
  const int so = xoff + KS * c.w * 1024;
#pragma unroll
  for (int pos = 0; pos < LA; ++pos)
#pragma unroll
    for (int u = 0; u < KS; ++u) { xf[pos][u] = ld16(c.ws, voff[pos], so + u * 1024); SB(); }
  if (STB >= 0) STAMP(STB);
  f4 acc[2][5], part[15];
  const LAS f4* src = (const LAS f4*)(lds + L_BIG + (c.w * 15) * 1024 + c.lane * 16);
#pragma unroll
  for (int pos = 0; pos < 4; ++pos) {
    LAS f4* dst = (LAS f4*)(lds + L_BIG + ((((c.w + pos) & 3) * 3 + (3 - pos)) * 5) * 1024 + c.lane * 16);
#pragma unroll
    for (int nt = 0; nt < 5; ++nt) acc[pos & 1][nt] = f4{0.f, 0.f, 0.f, 0.f};
#pragma unroll
    for (int u = 0; u < KS; ++u) {
      const v4u xc = xf[pos % LA][u];
#pragma unroll
      for (int nt = 0; nt < 5; ++nt) acc[pos & 1][nt] = mfma(wf[nt][u], xc, acc[pos & 1][nt]);
      if (pos + LA < 4) xf[pos % LA][u] = ld16(c.ws, voff[pos + LA], so + u * 1024);
      else if ((pos + LA - 4) * KS + u < NPRE) pre((pos + LA - 4) * KS + u);
      if (pos >= 1 && pos < 3 && u < 5) dst[u * 64] = acc[(pos - 1) & 1][u];
      if (pos == 3) {
#pragma unroll
        for (int e = 0; e < 15; ++e)
          if (e * KS / 15 == u) part[e] = src[e * 64];
      }
      __builtin_amdgcn_sched_group_barrier(0x008, 2, 0);
      __builtin_amdgcn_sched_group_barrier(0x020, 1, 0);
      __builtin_amdgcn_sched_group_barrier(0x008, 1, 0);
      __builtin_amdgcn_sched_group_barrier(0x080, 1, 0);
      __builtin_amdgcn_sched_group_barrier(0x008, 1, 0);
      __builtin_amdgcn_sched_group_barrier(0x080, 1, 0);
      __builtin_amdgcn_sched_group_barrier(0x008, 1, 0);
      __builtin_amdgcn_sched_group_barrier(0x080, 1, 0);
      SB();
    }
    if (pos == 2) {
      LAS f4* d2 = (LAS f4*)(lds + L_BIG + ((((c.w + 3) & 3) * 3 + 0) * 5) * 1024 + c.lane * 16);
#pragma unroll
      for (int nt = 0; nt < 5; ++nt) d2[nt * 64] = acc[0][nt];
      BAR();
      SB();
    }
    if (STB >= 0) STAMP(STB + 1 + pos);
  }
#pragma unroll
  for (int nt = 0; nt < 5; ++nt) out[nt] = (acc[1][nt] + part[nt]) + (part[5 + nt] + part[10 + nt]);
}

template <int NT, int NTT, int STEPS, int LK, class FX, class FE>
DI void gemm_lds(const LAS char* wl, f4 (&acc)[STEPS / 20][NT][NTT], FX xload, FE extra) {
  constexpr int D = NT <= 4 ? 2 : 1;
  v4u xf[LK][NTT];
#pragma unroll
  for (int t = 0; t < LK; ++t)
#pragma unroll
    for (int t2 = 0; t2 < NTT; ++t2) { xf[t][t2] = xload(t, t2); SB(); }
  v4u wb[D + 1][NT];
#pragma unroll
  for (int d = 0; d < D; ++d)
#pragma unroll
    for (int nt = 0; nt < NT; ++nt) wb[d][nt] = *(const LAS v4u*)(wl + (nt * 20 + d) * 1024);
#pragma unroll
  for (int gq = 0; gq < STEPS / 20; ++gq)
#pragma unroll
    for (int nt = 0; nt < NT; ++nt)
#pragma unroll
      for (int t2 = 0; t2 < NTT; ++t2) acc[gq][nt][t2] = f4{0.f, 0.f, 0.f, 0.f};
  SB();
#pragma unroll
  for (int t = 0; t < STEPS; ++t) {
#pragma unroll
    for (int nt = 0; nt < NT; ++nt) {
      if (t + D < STEPS) wb[(t + D) % (D + 1)][nt] = *(const LAS v4u*)(wl + (nt * 20 + (t + D) % 20) * 1024);
#pragma unroll
      for (int t2 = 0; t2 < NTT; ++t2) acc[t / 20][nt][t2] = mfma(wb[t % (D + 1)][nt], xf[t % LK][t2], acc[t / 20][nt][t2]);
    }
    if (t + LK < STEPS) {
#pragma unroll
      for (int t2 = 0; t2 < NTT; ++t2) xf[t % LK][t2] = xload(t + LK, t2);
    }
    extra(t);
#pragma unroll
    for (int nt = 0; nt < NT; ++nt) {
      __builtin_amdgcn_sched_group_barrier(0x008, NTT, 0);
      __builtin_amdgcn_sched_group_barrier(0x100, 1, 0);
      if (nt >= NT - 3) __builtin_amdgcn_sched_group_barrier(0x020, 1, 0);
    }
    SB();
  }
}

DI void store5(const Ctx& c, f4 (&v)[5], int aoff, int tt_g, int j, bool round_back) {
  unsigned u[10];
#pragma unroll
  for (int nt = 0; nt < 5; ++nt) {
    u[2 * nt] = pk2(v[nt][0], v[nt][1]);
    u[2 * nt + 1] = pk2(v[nt][2], v[nt][3]);
  }
  const int so = aoff + tt_g * TILE6 + 10 * j * 256;
  st16(c.ws, c.g * 256 + c.tok * 16, so, v4u{u[0], u[1], u[2], u[3]});
  st16(c.ws, c.g * 256 + c.tok * 16, so + 1024, v4u{u[4], u[5], u[6], u[7]});
  st8(c.ws, (c.g >> 1) * 256 + c.tok * 16 + (c.g & 1) * 8, so + 2048, v2u{u[8], u[9]});
  if (round_back) {
#pragma unroll
    for (int nt = 0; nt < 5; ++nt) v[nt] = f4{blo(u[2 * nt]), bhi(u[2 * nt]), blo(u[2 * nt + 1]), bhi(u[2 * nt + 1])};
  }
}
DI void store_stats(const Ctx& c, const f4 (&v)[5], int soff, int tt_g, int j) {
  float s = 0.f, q = 0.f;
#pragma unroll
  for (int nt = 0; nt < 5; ++nt)
#pragma unroll
    for (int i = 0; i < 4; ++i) { s += v[nt][i]; q = __builtin_fmaf(v[nt][i], v[nt][i], q); }
  xred2<false>(s, q);
  st8(c.ws, c.g == 0 ? (unsigned)(c.tok * 64 + j * 8) : OOB, soff + tt_g * 1024, v2u{asu(s), asu(q)});
}
DI void features(const f4& p0, float p4, float lr, unsigned (&ph)[5]) {
  const float pv[5] = {p0[0], p0[1], p0[2], p0[3], p4};
  float mx = __builtin_fmaxf(__builtin_fmaxf(__builtin_fmaxf(pv[0], pv[1]), pv[2]), __builtin_fmaxf(pv[3], pv[4]));
  float mn = -__builtin_fminf(__builtin_fminf(__builtin_fminf(pv[0], pv[1]), pv[2]), __builtin_fminf(pv[3], pv[4]));
  xred2<true>(mx, mn);
  const f2 sc = f2{lr, -lr}, of = f2{-mx * lr, -mn * lr};
  f2 e[5];
#pragma unroll
  for (int i = 0; i < 5; ++i) {
    const f2 a = f2{pv[i], pv[i]} * sc + of;
    e[i] = f2{__builtin_amdgcn_exp2f(a[0]), __builtin_amdgcn_exp2f(a[1])};
  }
  const f2 sm = ((e[0] + e[1]) + (e[2] + e[3])) + e[4];
  float sp = sm[0], sn = sm[1];
  xred2<false>(sp, sn);
  const f2 inv = f2{__builtin_amdgcn_rcpf(sp), __builtin_amdgcn_rcpf(sn)};
#pragma unroll
  for (int i = 0; i < 5; ++i) {
    const f2 y = e[i] * inv;
    ph[i] = pk2(y[0], y[1]);
  }
}

#define PHASE_IDS()                                                                     \
  Ctx c = c0;                                                                           \
  asm volatile("" : "+v"(c.lane));                                                      \
  asm volatile("" : "+s"(c.w));                                                         \
  c.tok = c.lane & 15;                                                                  \
  c.g = c.lane >> 4;                                                                    \
  int bb_ = blockIdx.x;                                                                 \
  asm volatile("" : "+s"(bb_));                                                         \
  const int xg = bb_ & 7, idx = bb_ >> 3;                                               \
  const int p = 4 * xg + (idx >> 3), j = idx & 7;                                       \
  const int G = xg, h = idx;                                                            \
  const int q6 = 2 * xg + (idx >> 4), i6 = idx & 15;                                    \
  const int w = c.w, lane = c.lane, tok = c.tok, g = c.g;                               \
  const int tid = w * 64 + lane;                                                        \
  const int ttg = 4 * p + w;                                                            \
  (void)p; (void)j; (void)G; (void)h; (void)q6; (void)i6; (void)tok; (void)g; (void)tid; (void)ttg; (void)lane;

__global__ void __launch_bounds__(256, 1) fused_kernel(Params P) {
  extern __shared__ __attribute__((aligned(16))) char smem[];
  LAS char* lds = (LAS char*)smem;
  Ctx c0;
  c0.wsp = P.ws;
  c0.ws = __builtin_amdgcn_make_buffer_rsrc((void*)P.ws, 0, WS_USED, 0x00020000);
  c0.lane = threadIdx.x & 63;
  c0.w = __builtin_amdgcn_readfirstlane(threadIdx.x >> 6);
  c0.tok = c0.lane & 15;
  c0.g = c0.lane >> 4;
  c0.tag = 0;
  STAMP(0);
  {
    const u64 t = __hip_atomic_load((GAS u64*)(P.ws + OFF_EP), __ATOMIC_RELAXED, __HIP_MEMORY_SCOPE_AGENT) + 1;
    c0.tag = ((u64)__builtin_amdgcn_readfirstlane((unsigned)(t >> 32)) << 32) | __builtin_amdgcn_readfirstlane((unsigned)t);
  }
  v4u wph[2];
  f4 x2[5], x1[5];
  {
  PHASE_IDS();
  const rsrc_t rx = __builtin_amdgcn_make_buffer_rsrc((void*)P.x, 0, SEQ * DM * 2, 0x00020000);
  stage_a(P, c, G, h);
  DRAIN();
  BAR();
  if (threadIdx.x == 0) {
    __hip_atomic_store(flag_ptr(c, FG_A1 + h, G), c.tag, __ATOMIC_RELAXED, __HIP_MEMORY_SCOPE_AGENT);
    __hip_atomic_store(flag_ptr(c, FG_A2 + G * 4 + (h >> 3), h & 7), c.tag, __ATOMIC_RELAXED, __HIP_MEMORY_SCOPE_AGENT);
  }
  STAMP(1);
  stage_b(P, c, xg * 32 + idx);
  DRAIN();
  BAR();
  if (threadIdx.x == 0) __hip_atomic_store((GAS u64*)(P.ws + OFF_FB) + blockIdx.x, c.tag, __ATOMIC_RELAXED, __HIP_MEMORY_SCOPE_AGENT);
  STAMP(2);

  {
    const int gp = tok >> 2, i = tok & 3;
#pragma unroll
    for (int R = 0; R < 2; ++R) {
      const bool rv = (R == 0) || (i == 0);
      const int f = R == 0 ? 5 * gp + i : 5 * gp + 4;
      unsigned short e[5];
#pragma unroll
      for (int jj = 0; jj < 5; ++jj) e[jj] = P.wphi[(h * 20 + 5 * g + jj) * 20 + f];
      wph[R] = rv ? v4u{e[0] | ((unsigned)e[1] << 16), e[2] | ((unsigned)e[3] << 16), (unsigned)e[4], 0u} : v4u{0u, 0u, 0u, 0u};
    }
  }
  {
    const unsigned vo = (unsigned)((16 * (4 * p + w) + tok) * (DM * 2) + 160 * j);
    const v4u a = ld16p(rx, vo + g * 16, 0), bq = ld16p(rx, vo + 64 + g * 16, 0);
    const v2u e = ld8p(rx, vo + 128 + g * 8, 0);
    x2[0] = f4{blo(a.x), bhi(a.x), blo(a.y), bhi(a.y)};
    x2[1] = f4{blo(a.z), bhi(a.z), blo(a.w), bhi(a.w)};
    x2[2] = f4{blo(bq.x), bhi(bq.x), blo(bq.y), bhi(bq.y)};
    x2[3] = f4{blo(bq.z), bhi(bq.z), blo(bq.w), bhi(bq.w)};
    x2[4] = f4{blo(e.x), bhi(e.x), blo(e.y), bhi(e.y)};
  }
  wait_flags(c, OFF_FLAG + (lane < 8 ? (FG_A1 + h) * 256 + lane * 8 : (FG_A2 + G * 4 + (((lane - 8) & 31) >> 3)) * 256 + ((lane - 8) & 7) * 8), lane < 40);
  {
    v4u t[20];
#pragma unroll
    for (int u = 0; u < 20; ++u) t[u] = ld16(c.ws, lane * 16, OFF_WQ + (h * 80 + w * 20 + u) * 1024);
    unsigned cp[8];
#pragma unroll
    for (int gg = 0; gg < 8; ++gg) cp[gg] = ld4(c.ws, lane * 4, OFF_CSQ + (h * 8 + gg) * 256);
    SB();
#pragma unroll
    for (int u = 0; u < 20; ++u) *(LAS v4u*)(lds + L_BIG + (w * 20 + u) * 1024 + lane * 16) = t[u];
    float cs = 0.f;
#pragma unroll
    for (int gg = 0; gg < 8; ++gg) cs += asf(cp[gg]);
    if (w == 0) *(LAS float*)(lds + L_CSQ + lane * 4) = cs;
  }
  }

#pragma unroll 1
  for (int L = 0; L < 2; ++L) {
    const int act = OFF_ACT + L * SZ_LAYER;
    const int fgl = FG_L + L * FG_PER_L;
    v4u wa[5][5], wb[5][5];
    v4u phq0[4];
    unsigned phq1[4];
    {
      PHASE_IDS();
      const int tt0 = 16 * G + 4 * w;
      if (L == 1) wait_group(c, FG_L + FG_X3 + 4 * G + w, 0, 8);
      STAMP(3 + 20 * L);
      const rsrc_t rX = __builtin_amdgcn_make_buffer_rsrc(L == 0 ? (void*)P.x : (void*)P.ws, 0, L == 0 ? SEQ * DM * 2 : WS_USED, 0x00020000);
      const unsigned xv0 = L == 0 ? (unsigned)((16 * tt0 + tok) * (DM * 2) + g * 16) : (unsigned)(tt0 * TILE6 + lane * 16);
      const int xvs = L == 0 ? 16 * DM * 2 : TILE6, xs0 = L == 0 ? 0 : OFF_ACT + A_X3, xss = L == 0 ? 64 : 1024;
      v4u stv[4];
      {
        const unsigned sv0 = L == 0 ? (unsigned)(((16 * tt0 + tok) * 8) & ~15) : (unsigned)(tt0 * 1024 + tok * 64 + g * 16);
        const int svs = L == 0 ? 128 : 1024, sso = L == 0 ? OFF_ST0 : OFF_ACT + A_S3;
#pragma unroll
        for (int tt = 0; tt < 4; ++tt) stv[tt] = ld16(c.ws, sv0 + tt * svs, sso);
      }
      SB();
      BAR();
      f4 acc2[2][4][2];
      gemm_lds<4, 2, 40, 8>(lds + L_BIG + lane * 16, acc2,
                            [&](int t, int t2) { return ld16(rX, xv0 + (2 * (t / 20) + t2) * xvs, xs0 + (t % 20) * xss); }, [&](int) {});
      f4 acc[4][4];
#pragma unroll
      for (int nt = 0; nt < 4; ++nt) { acc[nt][0] = acc2[0][nt][0]; acc[nt][1] = acc2[0][nt][1]; acc[nt][2] = acc2[1][nt][0]; acc[nt][3] = acc2[1][nt][1]; }
      if (L == 0) {
        GAS u64* ab = (GAS u64*)(c.wsp + OFF_ABORT);
        for (unsigned spins = 0;;) {
          const v4u a = ld16(c.ws, lane * 32, OFF_FB), bq = ld16(c.ws, lane * 32 + 16, OFF_FB);
          const unsigned lo = (unsigned)c.tag, hi = (unsigned)(c.tag >> 32);
          const bool ok = a.x == lo && a.y == hi && a.z == lo && a.w == hi && bq.x == lo && bq.y == hi && bq.z == lo && bq.w == hi;
          if (__all(ok)) break;
          ++spins;
          if ((spins & 63u) == 0u) {
            u64 av = __hip_atomic_load(ab, __ATOMIC_RELAXED, __HIP_MEMORY_SCOPE_AGENT);
            if (__any(av == c.tag)) break;
            if (spins > SPIN_CAP) { __hip_atomic_store(ab, c.tag, __ATOMIC_RELAXED, __HIP_MEMORY_SCOPE_AGENT); break; }
          }
        }
        __builtin_amdgcn_fence(__ATOMIC_ACQUIRE, "wavefront");
        asm volatile("" ::: "memory");
      }
      STAMP(4 + 20 * L);
      float mean[4], lr[4], rstd[4];
#pragma unroll
      for (int tt = 0; tt < 4; ++tt) {
        float sm, sq;
        if (L == 0) {
          const bool hi = tok & 1;
          sm = asf(hi ? stv[tt].z : stv[tt].x);
          sq = asf(hi ? stv[tt].w : stv[tt].y);
        } else {
          const f4 v = asf4(stv[tt]);
          sm = v[0] + v[2];
          sq = v[1] + v[3];
          xred2<false>(sm, sq);
        }
        mean[tt] = sm * (1.0f / DM);
        const float var = __builtin_fmaxf(sq * (1.0f / DM) - mean[tt] * mean[tt], 0.f);
        rstd[tt] = __builtin_amdgcn_rsqf(var + LN_EPS);
        lr[tt] = rstd[tt] * LOG2E;
      }
      f4 csq[4];
#pragma unroll
      for (int nt = 0; nt < 4; ++nt) csq[nt] = *(const LAS f4*)(lds + L_CSQ + (nt * 16 + 4 * g) * 4);
      const f4 z4 = f4{0.f, 0.f, 0.f, 0.f};
      {
        f4 pk0[4], pk1[4];
        float val[4][16];
#pragma unroll
        for (int tt = 0; tt < 4; ++tt) {
#pragma unroll
          for (int v = 5; v < 15; ++v) val[tt][v] = __builtin_fmaf(-mean[tt], csq[v >> 2][v & 3], acc[v >> 2][tt][v & 3]);
          const v4u bq = v4u{pk2(val[tt][5], val[tt][6]), pk2(val[tt][7], val[tt][8]), pk2(val[tt][9], 0.f), 0u};
          pk0[tt] = mfma(wph[0], bq, z4);
          pk1[tt] = mfma(wph[1], bq, z4);
        }
#pragma unroll
        for (int tt = 0; tt < 4; ++tt) {
          unsigned ph[5];
          features(pk0[tt], pk1[tt][0], lr[tt], ph);
          const float r = rstd[tt];
          LAS char* row = lds + L_SROW + (64 * w + 16 * tt + tok) * SROW;
          *(LAS v2u*)(row + g * 24) = v2u{ph[0], ph[1]};
          *(LAS v2u*)(row + g * 24 + 8) = v2u{ph[2], ph[3]};
          *(LAS v2u*)(row + g * 24 + 16) = v2u{ph[4], 0u};
          *(LAS v2u*)(row + 96 + g * 16) = v2u{pk2(val[tt][10] * r, val[tt][11] * r), pk2(val[tt][12] * r, val[tt][13] * r)};
          *(LAS v2u*)(row + 96 + g * 16 + 8) = v2u{pk2(val[tt][14] * r, 1.0f), 0u};
        }
      }
      BAR();
      {
        f4 kv[3][2];
        const int qq = tok >> 2, pp = tok & 3;
        v4u fa[2][3], fb[2][2];
#pragma unroll
        for (int ks = 0; ks < 2; ++ks) {
          const LAS char* r0 = lds + L_SROW + (64 * w + 32 * ks + 8 * g + qq) * SROW + pp * 8;
#pragma unroll
          for (int a = 0; a < 3; ++a) {
            const s4 lo = __builtin_amdgcn_ds_read_tr16_b64_v4i16((LAS s4*)(r0 + a * 32));
            const s4 hi = __builtin_amdgcn_ds_read_tr16_b64_v4i16((LAS s4*)(r0 + 4 * SROW + a * 32));
            fa[ks][a] = __builtin_bit_cast(v4u, __builtin_shufflevector(lo, hi, 0, 1, 2, 3, 4, 5, 6, 7));
          }
#pragma unroll
          for (int bt = 0; bt < 2; ++bt) {
            const s4 lo = __builtin_amdgcn_ds_read_tr16_b64_v4i16((LAS s4*)(r0 + 96 + bt * 32));
            const s4 hi = __builtin_amdgcn_ds_read_tr16_b64_v4i16((LAS s4*)(r0 + 4 * SROW + 96 + bt * 32));
            fb[ks][bt] = __builtin_bit_cast(v4u, __builtin_shufflevector(lo, hi, 0, 1, 2, 3, 4, 5, 6, 7));
          }
        }
        SB();
#pragma unroll
        for (int a = 0; a < 3; ++a)
#pragma unroll
          for (int bt = 0; bt < 2; ++bt) {
            kv[a][bt] = mfma(fa[0][a], fb[0][bt], z4);
            kv[a][bt] = mfma(fa[1][a], fb[1][bt], kv[a][bt]);
          }
#pragma unroll
        for (int a = 0; a < 3; ++a)
#pragma unroll
          for (int bt = 0; bt < 2; ++bt) *(LAS f4*)(lds + L_KVW + (w * 6 + a * 2 + bt) * 1024 + lane * 16) = kv[a][bt];
      }
      BAR();
      {
        const int e1 = 256 + (tid & 127);
        f4 t0[4], t1[4];
#pragma unroll
        for (int ww = 0; ww < 4; ++ww) {
          t0[ww] = *(const LAS f4*)(lds + L_KVW + ww * 6144 + tid * 16);
          t1[ww] = *(const LAS f4*)(lds + L_KVW + ww * 6144 + e1 * 16);
        }
        SB();
        st16(c.ws, tid * 16, act + A_KVP + (h * 8 + G) * 6144, asu4((t0[0] + t0[1]) + (t0[2] + t0[3])));
        st16(c.ws, tid < 128 ? (unsigned)(e1 * 16) : OOB, act + A_KVP + (h * 8 + G) * 6144, asu4((t1[0] + t1[1]) + (t1[2] + t1[3])));
      }
      publish(c, fgl + FG_KV + h, G);
      STAMP(5 + 20 * L);
      load_w5<5>(c, OFF_WO, j, wa);
      {
        f4 pq0[4], pq1[4];
#pragma unroll
        for (int tt = 0; tt < 4; ++tt) {
          float v[5];
#pragma unroll
          for (int i = 0; i < 5; ++i) v[i] = __builtin_fmaf(-mean[tt], csq[i >> 2][i & 3], acc[i >> 2][tt][i & 3]);
          const v4u bq = v4u{pk2(v[0], v[1]), pk2(v[2], v[3]), pk2(v[4], 0.f), 0u};
          pq0[tt] = mfma(wph[0], bq, z4);
          pq1[tt] = mfma(wph[1], bq, z4);
        }
#pragma unroll
        for (int tt = 0; tt < 4; ++tt) {
          unsigned ph[5];
          features(pq0[tt], pq1[tt][0], lr[tt], ph);
          phq0[tt] = v4u{ph[0], ph[1], ph[2], ph[3]};
          phq1[tt] = ph[4];
        }
      }
    }
    {
      PHASE_IDS();
      wait_group(c, fgl + FG_KV + h, 0, 8);
      STAMP(6 + 20 * L);
      {
        const int e1 = 256 + (tid & 127);
        v4u t0[8], t1[8];
#pragma unroll
        for (int gg = 0; gg < 8; ++gg) {
          t0[gg] = ld16(c.ws, tid * 16, act + A_KVP + (h * 8 + gg) * 6144);
          t1[gg] = ld16(c.ws, tid < 128 ? (unsigned)(e1 * 16) : OOB, act + A_KVP + (h * 8 + gg) * 6144);
        }
        SB();
        if (L == 0) {
          const int t = tid & 127;
          const bool three = tid >= 128;
          const bool on = t < (three ? 96 : 80);
          const unsigned vo = on ? (unsigned)(((three ? 96 * i6 : 80 * j) + t) * 4 + (three ? OFF_CS3 - OFF_CS1 : 0)) : OOB;
          const int st = three ? 1536 * 4 : 640 * 4;
          unsigned cp[20];
#pragma unroll
          for (int kq = 0; kq < 20; ++kq) cp[kq] = ld4(c.ws, vo + (on ? kq * st : 0), OFF_CS1);
          SB();
          float cs = 0.f;
#pragma unroll
          for (int kq = 0; kq < 20; ++kq) cs += asf(cp[kq]);
          if (on) *(LAS float*)(lds + (three ? L_CS3 : L_CS1) + t * 4) = cs;
        }
#pragma unroll
        for (int rep = 0; rep < 2; ++rep) {
          const v4u* t = rep ? t1 : t0;
          const f4 s = ((asf4(t[0]) + asf4(t[1])) + (asf4(t[2]) + asf4(t[3]))) + ((asf4(t[4]) + asf4(t[5])) + (asf4(t[6]) + asf4(t[7])));
          const int e = rep ? e1 : tid;
          const int tl = e >> 6, ln = e & 63;
          const int a = tl >> 1, bt = tl & 1;
          LAS float* dst = (LAS float*)(lds + L_KV) + (16 * a + 4 * (ln >> 4)) * 33 + 16 * bt + (ln & 15);
          if (rep == 0 || tid < 128) {
#pragma unroll
            for (int i = 0; i < 4; ++i) dst[i * 33] = s[i];
          }
        }
      }
      BAR();
      v4u ka[3][2];
      const bool odd = h & 1;
      {
        const int gp = tok >> 2, i = tok & 3;
        float e[3][10];
        bool rv[3];
#pragma unroll
        for (int R = 0; R < 3; ++R) {
          int d, bs;
          if (R < 2) {
            d = odd ? (gp == 0 ? i : 8 * gp - 4 + 4 * R + i) : 8 * gp + 4 * R + i;
            rv[R] = odd ? (gp == 0 ? R == 0 : gp < 3) : d < 20;
            d = rv[R] ? d : 0;
            bs = 8 * (d / 5) + d % 5;
          } else {
            rv[R] = true;
            bs = 5;
          }
          const LAS float* src = (const LAS float*)(lds + L_KV) + (12 * g) * 33 + bs;
#pragma unroll
          for (int jj = 0; jj < 10; ++jj) e[R][jj] = src[jj * 33];
        }
        SB();
#pragma unroll
        for (int R = 0; R < 3; ++R) {
          const unsigned m = rv[R] ? 0xffffffffu : 0u;
          ka[R][0] = v4u{pk2(e[R][0], e[R][1]) & m, pk2(e[R][2], e[R][3]) & m, pk2(e[R][4], e[R][5]) & m, pk2(e[R][6], e[R][7]) & m};
          ka[R][1] = v4u{pk2(e[R][8], e[R][9]) & m, 0u, 0u, 0u};
        }
      }
      const int n0 = (20 * h) >> 3;
      f4 o[4][3];
#pragma unroll
      for (int tt = 0; tt < 4; ++tt) {
        const v4u b1 = v4u{phq1[tt], 0u, 0u, 0u};
#pragma unroll
        for (int R = 0; R < 3; ++R) {
          o[tt][R] = mfma(ka[R][0], phq0[tt], f4{0.f, 0.f, 0.f, 0.f});
          o[tt][R] = mfma(ka[R][1], b1, o[tt][R]);
        }
      }
      const bool full = odd ? (g == 1 || g == 2) : (g < 2);
      const bool half = odd ? (g == 0) : (g == 2);
      const unsigned vfull = full ? (unsigned)(g * 256 + tok * 16) : OOB, vhalf = half ? (unsigned)(g * 256 + tok * 16 + (odd ? 8 : 0)) : OOB;
#pragma unroll
      for (int tt = 0; tt < 4; ++tt) {
        const float inv = __builtin_amdgcn_rcpf(o[tt][2][0] + 1e-6f);
        const unsigned u0 = pk2(o[tt][0][0] * inv, o[tt][0][1] * inv), u1 = pk2(o[tt][0][2] * inv, o[tt][0][3] * inv);
        const unsigned u2 = pk2(o[tt][1][0] * inv, o[tt][1][1] * inv), u3 = pk2(o[tt][1][2] * inv, o[tt][1][3] * inv);
        const int so = act + A_O + (16 * G + 4 * w + tt) * TILE6 + n0 * 256;
        st16(c.ws, vfull, so, v4u{u0, u1, u2, u3});
        st8(c.ws, vhalf, so, v2u{u0, u1});
      }
      publish(c, fgl + FG_O + G * 4 + (h >> 3), h & 7);
      STAMP(7 + 20 * L);
    }
    {
      PHASE_IDS();
      wait_group(c, fgl + FG_O + (p >> 2) * 4 + w, 0, 8);
      STAMP(8 + 20 * L);
      f4 o[5];
      gemm5<5, 2, 10, 44>(c, lds, act + A_O, TILE6, p, 0, wa, o, [&](int i) { wb[i % 5][i / 5] = ld16(c.ws, lane * 16, OFF_W1 + ((5 * j + i % 5) * 80 + 4 * (5 * w + i / 5)) * 256); });
#pragma unroll
      for (int nt = 0; nt < 5; ++nt) x2[nt] += o[nt];
      STAMP(50);
      store5(c, x2, act + A_X2, ttg, j, true);
      store_stats(c, x2, act + A_S2, ttg, j);
      STAMP(51);
      DRAIN();
      STAMP(52);
      publish(c, fgl + FG_X2 + p, j);
#pragma unroll
      for (int i = 10; i < 25; ++i) wb[i % 5][i / 5] = ld16(c.ws, lane * 16, OFF_W1 + ((5 * j + i % 5) * 80 + 4 * (5 * w + i / 5)) * 256);
      STAMP(9 + 20 * L);
    }
    float m2, r2;
    {
      PHASE_IDS();
      wait_group(c, fgl + FG_X2 + p, 0, 8);
      STAMP(10 + 20 * L);
      const v4u sv = ld16(c.ws, tok * 64 + g * 16, act + A_S2 + ttg * 1024);
      f4 o[5];
      gemm5<5, 2, 10, -1>(c, lds, act + A_X2, TILE6, p, 0, wb, o, [&](int i) { wa[i % 5][i / 5] = ld16(c.ws, lane * 16, OFF_W2 + ((5 * j + i % 5) * 80 + 4 * (5 * w + i / 5)) * 256); });
      {
        float s = asf(sv.x) + asf(sv.z), q = asf(sv.y) + asf(sv.w);
        xred2<false>(s, q);
        m2 = s * (1.0f / DM);
        const float var = __builtin_fmaxf(q * (1.0f / DM) - m2 * m2, 0.f);
        r2 = __builtin_amdgcn_rsqf(var + LN_EPS);
        const float rr = r2 * __builtin_amdgcn_rsqf(var * r2 * r2 + LN_EPS);
#pragma unroll
        for (int nt = 0; nt < 5; ++nt) {
          const f4 cs = *(const LAS f4*)(lds + L_CS1 + (nt * 16 + 4 * g) * 4);
#pragma unroll
          for (int i = 0; i < 4; ++i) o[nt][i] = gelu(rr * __builtin_fmaf(-m2, cs[i], o[nt][i]));
        }
      }
      store5(c, o, act + A_H, ttg, j, false);
      publish(c, fgl + FG_H + p, j);
#pragma unroll
      for (int i = 10; i < 25; ++i) wa[i % 5][i / 5] = ld16(c.ws, lane * 16, OFF_W2 + ((5 * j + i % 5) * 80 + 4 * (5 * w + i / 5)) * 256);
      STAMP(11 + 20 * L);
    }
    {
      PHASE_IDS();
      const int shift = w == 0 ? -1 : (w == 1 ? 1 : (w == 2 ? -2 : 2));
      const int pn = p + (shift < 0 ? -1 : 1);
      const bool nv = pn >= 0 && pn < 32;
      wait_flags(c, OFF_FLAG + (fgl + FG_H + ((lane & 2) && nv ? pn : p)) * 256 + (2 * w + (lane & 1)) * 8, lane < 4);
      STAMP(12 + 20 * L);
      f4 o[5];
      v4u t3[30];
      gemm5<5, 2, 10, -1>(c, lds, act + A_H, TILE6, p, shift, wa, o, [&](int u) { t3[u] = ld16(c.ws, lane * 16, OFF_W3 + (i6 * 120 + w * 30 + u) * 1024); });
#pragma unroll
      for (int nt = 0; nt < 5; ++nt)
#pragma unroll
        for (int i = 0; i < 4; ++i) x1[nt][i] = (x2[nt][i] - m2) * r2 + o[nt][i];
      store5(c, x1, act + A_X1, ttg, j, true);
      store_stats(c, x1, act + A_S1, ttg, j);
      publish(c, fgl + FG_X1 + p, j);
#pragma unroll
      for (int u = 10; u < 30; ++u) t3[u] = ld16(c.ws, lane * 16, OFF_W3 + (i6 * 120 + w * 30 + u) * 1024);
      SB();
#pragma unroll
      for (int u = 0; u < 30; ++u) *(LAS v4u*)(lds + L_BIG + (w * 30 + u) * 1024 + lane * 16) = t3[u];
      STAMP(13 + 20 * L);
    }
    v4u w4f[5][12];
    {
      PHASE_IDS();
      const int tt0 = 8 * q6 + 2 * w;
      wait_group(c, fgl + FG_X1 + 2 * q6 + (w >> 1), 0, 8);
      STAMP(14 + 20 * L);
      v4u sv[2];
#pragma unroll
      for (int t2 = 0; t2 < 2; ++t2) sv[t2] = ld16(c.ws, tok * 64 + g * 16, act + A_S1 + (tt0 + t2) * 1024);
      SB();
      BAR();
      f4 acc1[1][6][2];
      gemm_lds<6, 2, 20, 6>(lds + L_BIG + lane * 16, acc1,
                            [&](int t, int t2) { return ld16(c.ws, lane * 16, act + A_X1 + (tt0 + t2) * TILE6 + t * 1024); },
                            [&](int t) {
                              const int e0 = t < 14 ? t : 14 + 3 * (t - 14), n = t < 14 ? 1 : 3;
#pragma unroll
                              for (int e = e0; e < e0 + n; ++e)
                                w4f[e % 5][e / 5] = ld16(c.ws, lane * 16, OFF_W4 + ((5 * j + e % 5) * 192 + 4 * (12 * w + e / 5)) * 256);
                            });
      const auto& acc = acc1[0];
#pragma unroll
      for (int t2 = 0; t2 < 2; ++t2) {
        float s = asf(sv[t2].x) + asf(sv[t2].z), q = asf(sv[t2].y) + asf(sv[t2].w);
        xred2<false>(s, q);
        const float mean = s * (1.0f / DM);
        const float rstd = __builtin_amdgcn_rsqf(__builtin_fmaxf(q * (1.0f / DM) - mean * mean, 0.f) + LN_EPS);
#pragma unroll
        for (int a = 0; a < 3; ++a) {
          unsigned u[4];
#pragma unroll
          for (int hf = 0; hf < 2; ++hf) {
            const int nt = 2 * a + hf;
            const f4 cs = *(const LAS f4*)(lds + L_CS3 + (nt * 16 + 4 * g) * 4);
            float y[4];
#pragma unroll
            for (int i = 0; i < 4; ++i) y[i] = gelu(rstd * __builtin_fmaf(-mean, cs[i], acc[nt][t2][i]));
            u[2 * hf] = pk2(y[0], y[1]);
            u[2 * hf + 1] = pk2(y[2], y[3]);
          }
          st16(c.ws, g * 256 + tok * 16, act + A_H2 + (tt0 + t2) * TILEF + (12 * i6 + 4 * a) * 256, v4u{u[0], u[1], u[2], u[3]});
        }
      }
      publish(c, fgl + FG_H2 + q6 * 2 + (i6 >> 3), i6 & 7);
#pragma unroll
      for (int e = 32; e < 60; ++e) w4f[e % 5][e / 5] = ld16(c.ws, lane * 16, OFF_W4 + ((5 * j + e % 5) * 192 + 4 * (12 * w + e / 5)) * 256);
      STAMP(15 + 20 * L);
    }
    {
      PHASE_IDS();
      wait_group(c, fgl + FG_H2 + (p >> 1) * 2 + (w >> 1), 4 * (w & 1), 4);
      STAMP(16 + 20 * L);
      f4 o[5];
      v4u tq[20];
      gemm5<12, 1, 12, -1>(c, lds, act + A_H2, TILEF, p, 0, w4f, o, [&](int u) { tq[u] = ld16(c.ws, lane * 16, OFF_WQ + (h * 80 + w * 20 + u) * 1024); });
#pragma unroll
      for (int nt = 0; nt < 5; ++nt) x2[nt] += x1[nt] + o[nt];
      if (L == 0) {
        store5(c, x2, act + A_X3, ttg, j, true);
        store_stats(c, x2, act + A_S3, ttg, j);
        publish(c, fgl + FG_X3 + p, j);
#pragma unroll
        for (int u = 12; u < 20; ++u) tq[u] = ld16(c.ws, lane * 16, OFF_WQ + (h * 80 + w * 20 + u) * 1024);
        SB();
#pragma unroll
        for (int u = 0; u < 20; ++u) *(LAS v4u*)(lds + L_BIG + (w * 20 + u) * 1024 + lane * 16) = tq[u];
      } else {
        const rsrc_t ro = __builtin_amdgcn_make_buffer_rsrc((void*)P.out, 0, SEQ * DM * 2, 0x00020000);
        const unsigned vo = (unsigned)((16 * ttg + tok) * (DM * 2) + 160 * j);
        __builtin_amdgcn_raw_buffer_store_b128(v4u{pk2(x2[0][0], x2[0][1]), pk2(x2[0][2], x2[0][3]), pk2(x2[1][0], x2[1][1]), pk2(x2[1][2], x2[1][3])},
                                               ro, vo + g * 16, 0, 0);
        __builtin_amdgcn_raw_buffer_store_b128(v4u{pk2(x2[2][0], x2[2][1]), pk2(x2[2][2], x2[2][3]), pk2(x2[3][0], x2[3][1]), pk2(x2[3][2], x2[3][3])},
                                               ro, vo + 64 + g * 16, 0, 0);
        __builtin_amdgcn_raw_buffer_store_b64(v2u{pk2(x2[4][0], x2[4][1]), pk2(x2[4][2], x2[4][3])}, ro, vo + 128 + g * 8, 0, 0);
      }
      STAMP(17 + 20 * L);
    }
  }
  if (blockIdx.x == 0 && threadIdx.x == 0) __hip_atomic_store((GAS u64*)(P.ws + OFF_EP), c0.tag, __ATOMIC_RELAXED, __HIP_MEMORY_SCOPE_AGENT);
}

}

extern "C" void kernel_launch(void* const* d_in, const int* in_sizes, int n_in, void* d_out, int out_size, void* d_ws, size_t ws_size,
                              hipStream_t stream) {
  (void)in_sizes; (void)n_in; (void)out_size; (void)ws_size;
  hk::Params P;
  P.x = (const unsigned short*)d_in[0];
  P.wqkv = (const unsigned short*)d_in[1];
  P.wphi = (const unsigned short*)d_in[2];
  P.wo = (const unsigned short*)d_in[3];
  P.w1 = (const unsigned short*)d_in[5];
  P.w2 = (const unsigned short*)d_in[6];
  P.w3 = (const unsigned short*)d_in[7];
  P.w4 = (const unsigned short*)d_in[8];
  P.out = (unsigned short*)d_out;
  P.ws = (char*)d_ws;
  (void)hipFuncSetAttribute((const void*)hk::fused_kernel, hipFuncAttributeMaxDynamicSharedMemorySize, hk::LDS_BYTES);
  hk::fused_kernel<<<256, 256, hk::LDS_BYTES, stream>>>(P);
}
```

```cpp
#include <hip/hip_runtime.h>
#include <stdint.h>

namespace hk {

#define DI __device__ __forceinline__
#define GAS __attribute__((address_space(1)))
#define LAS __attribute__((address_space(3)))
typedef unsigned v4u __attribute__((ext_vector_type(4)));
typedef unsigned v2u __attribute__((ext_vector_type(2)));
typedef float f4 __attribute__((ext_vector_type(4)));
typedef float f2 __attribute__((ext_vector_type(2)));
typedef short s8 __attribute__((ext_vector_type(8)));
typedef short s4 __attribute__((ext_vector_type(4)));
typedef __bf16 bf2 __attribute__((ext_vector_type(2)));
typedef __amdgpu_buffer_rsrc_t rsrc_t;
typedef unsigned long long u64;

constexpr int SEQ = 2048, DM = 640, FF = 1536;
constexpr float LN_EPS = 1e-5f;
constexpr float LOG2E = 1.4426950408889634f;

constexpr int OFF_EP = 0;
constexpr int OFF_ABORT = 256;
constexpr int OFF_FB = 4096;
constexpr int OFF_FLAG = 8192;
constexpr int FG_A1 = 0;
constexpr int FG_A2 = 32;
constexpr int FG_L = 64;
constexpr int FG_KV = 0, FG_O = 32, FG_X2 = 64, FG_H = 96, FG_X1 = 128, FG_H2 = 160, FG_X3 = 192, FG_PER_L = 224;
constexpr int OFF_WQ = 1 << 18;
constexpr int SZ_WQ = 32 * 4 * 80 * 256;
constexpr int OFF_WO = OFF_WQ + SZ_WQ;
constexpr int SZ_W6 = 40 * 80 * 256;
constexpr int OFF_W1 = OFF_WO + SZ_W6;
constexpr int OFF_W2 = OFF_W1 + SZ_W6;
constexpr int OFF_W3 = OFF_W2 + SZ_W6;
constexpr int SZ_W3 = 96 * 80 * 256;
constexpr int OFF_W4 = OFF_W3 + SZ_W3;
constexpr int SZ_W4 = 40 * 192 * 256;
constexpr int OFF_CSQ = OFF_W4 + SZ_W4;
constexpr int OFF_CS1 = OFF_CSQ + 32 * 8 * 64 * 4;
constexpr int OFF_CS3 = OFF_CS1 + 20 * 640 * 4;
constexpr int TILE6 = 80 * 256;
constexpr int TILEF = 192 * 256;
constexpr int SZ_A6 = 128 * TILE6, SZ_AF = 128 * TILEF;
constexpr int SZ_ST = 128 * 1024;
constexpr int SZ_KVP = 32 * 8 * 6144;
constexpr int OFF_KVP = OFF_CS3 + 20 * 1536 * 4;
constexpr int OFF_ACT = OFF_KVP + 2 * SZ_KVP;
constexpr int A_O = 0, A_X2 = A_O + SZ_A6, A_H = A_X2 + SZ_A6, A_X1 = A_H + SZ_A6, A_X3 = A_X1 + SZ_A6, A_H2 = A_X3 + SZ_A6,
              A_S2 = A_H2 + SZ_AF, A_S1 = A_S2 + SZ_ST, A_S3 = A_S1 + SZ_ST, SZ_LAYER = A_S3 + SZ_ST;
constexpr int LAYER_STRIDE = 0;
constexpr int OFF_PROF = OFF_ACT + SZ_LAYER + LAYER_STRIDE;
constexpr int WS_USED = OFF_PROF + 256 * 64 * 8;
constexpr unsigned OOB = 0x80000000u;

constexpr int L_BIG = 0;
constexpr int L_RED = 101376;
constexpr int L_SROW = 81920;
constexpr int SROW = 168;
constexpr int L_KVW = 0;
constexpr int L_KB = 32768;
constexpr int L_XROW = 81920;
constexpr int L_AROW = 98304;
constexpr int L_BROW = 112640;
constexpr int L_SMALL = 162816;
constexpr int L_CSQ = L_SMALL;
constexpr int L_CS1 = L_CSQ + 256;
constexpr int L_CS3 = L_CS1 + 320;
constexpr int LDS_BYTES = 163840;

constexpr int CODE_LINES = 380;
constexpr int AUX_SC1 = 16;
constexpr unsigned SPIN_CAP = 1u << 17;

struct Params {
  const unsigned short* x;
  const unsigned short* wqkv;
  const unsigned short* wphi;
  const unsigned short* wo;
  const unsigned short* w1;
  const unsigned short* w2;
  const unsigned short* w3;
  const unsigned short* w4;
  unsigned short* out;
  char* ws;
};

DI f4 mfma(v4u a, v4u b, f4 c) {
  return __builtin_amdgcn_mfma_f32_16x16x32_bf16(__builtin_bit_cast(s8, a), __builtin_bit_cast(s8, b), c, 0, 0, 0);
}
DI unsigned pk2(float a, float b) {
  bf2 v = {(__bf16)a, (__bf16)b};
  return __builtin_bit_cast(unsigned, v);
}
DI float blo(unsigned u) { return __builtin_bit_cast(float, u << 16); }
DI float bhi(unsigned u) { return __builtin_bit_cast(float, u & 0xffff0000u); }
DI v4u ld16(rsrc_t r, unsigned voff, int soff) { return __builtin_amdgcn_raw_buffer_load_b128(r, voff, soff, AUX_SC1); }
DI v2u ld8(rsrc_t r, unsigned voff, int soff) { return __builtin_amdgcn_raw_buffer_load_b64(r, voff, soff, AUX_SC1); }
DI unsigned ld4(rsrc_t r, unsigned voff, int soff) { return __builtin_amdgcn_raw_buffer_load_b32(r, voff, soff, AUX_SC1); }
DI v4u ld16p(rsrc_t r, unsigned voff, int soff) { return __builtin_amdgcn_raw_buffer_load_b128(r, voff, soff, 0); }
DI v2u ld8p(rsrc_t r, unsigned voff, int soff) { return __builtin_amdgcn_raw_buffer_load_b64(r, voff, soff, 0); }
DI void st16(rsrc_t r, unsigned voff, int soff, v4u v) { __builtin_amdgcn_raw_buffer_store_b128(v, r, voff + (unsigned)soff, 0, AUX_SC1); }
DI void st8(rsrc_t r, unsigned voff, int soff, v2u v) { __builtin_amdgcn_raw_buffer_store_b64(v, r, voff, soff, AUX_SC1); }
DI void st4(rsrc_t r, unsigned voff, int soff, unsigned v) { __builtin_amdgcn_raw_buffer_store_b32(v, r, voff, soff, AUX_SC1); }
DI void dma16(rsrc_t r, LAS char* dst, unsigned voff, int soff) { __builtin_amdgcn_raw_ptr_buffer_load_lds(r, (LAS void*)dst, 16, voff, soff, 0, AUX_SC1); }
DI float asf(unsigned u) { return __builtin_bit_cast(float, u); }
DI unsigned asu(float f) { return __builtin_bit_cast(unsigned, f); }
DI f4 asf4(v4u u) { return __builtin_bit_cast(f4, u); }
DI v4u asu4(f4 f) { return __builtin_bit_cast(v4u, f); }

DI float xsum(float x) {
  auto a = __builtin_amdgcn_permlane16_swap(asu(x), asu(x), false, false);
  x = asf(a[0]) + asf(a[1]);
  auto b = __builtin_amdgcn_permlane32_swap(asu(x), asu(x), false, false);
  return asf(b[0]) + asf(b[1]);
}
DI void xsum2(float& X, float& Y) {
  auto a = __builtin_amdgcn_permlane32_swap(asu(X), asu(Y), false, false);
  float c = asf(a[0]) + asf(a[1]);
  auto d = __builtin_amdgcn_permlane16_swap(asu(c), asu(c), false, false);
  float e = asf(d[0]) + asf(d[1]);
  auto f = __builtin_amdgcn_permlane32_swap(asu(e), asu(e), false, false);
  X = asf(f[0]);
  Y = asf(f[1]);
}
DI void xmax2(float& X, float& Y, float inf) {
  auto a = __builtin_amdgcn_permlane32_swap(asu(X), asu(Y), false, false);
  float c = __builtin_amdgcn_fmed3f(asf(a[0]), asf(a[1]), inf);
  auto d = __builtin_amdgcn_permlane16_swap(asu(c), asu(c), false, false);
  float e = __builtin_amdgcn_fmed3f(asf(d[0]), asf(d[1]), inf);
  auto f = __builtin_amdgcn_permlane32_swap(asu(e), asu(e), false, false);
  X = asf(f[0]);
  Y = asf(f[1]);
}
struct GeluK { f2 c0, c1, one; };
DI GeluK gelu_consts() {
  GeluK k = {f2{-2.302208198f, -2.302208198f}, f2{-0.1029432397f, -0.1029432397f}, f2{1.0f, 1.0f}};
  asm volatile("" : "+v"(k.c0), "+v"(k.c1), "+v"(k.one));
  return k;
}
DI f2 gelu2(f2 x, const GeluK& k) {
  const f2 t = __builtin_elementwise_fma(x * x, k.c1, k.c0);
  const f2 a = x * t;
  const f2 d = f2{__builtin_amdgcn_exp2f(a[0]), __builtin_amdgcn_exp2f(a[1])} + k.one;
  return x * f2{__builtin_amdgcn_rcpf(d[0]), __builtin_amdgcn_rcpf(d[1])};
}
template <int N> DI void gelu_n(f2 (&x)[N], const GeluK& k) {
  f2 a[N];
#pragma unroll
  for (int i = 0; i < N; ++i) a[i] = x[i] * __builtin_elementwise_fma(x[i] * x[i], k.c1, k.c0);
#pragma unroll
  for (int i = 0; i < N; ++i) a[i] = f2{__builtin_amdgcn_exp2f(a[i][0]), __builtin_amdgcn_exp2f(a[i][1])};
#pragma unroll
  for (int i = 0; i < N; ++i) a[i] += k.one;
#pragma unroll
  for (int i = 0; i < N; ++i) a[i] = f2{__builtin_amdgcn_rcpf(a[i][0]), __builtin_amdgcn_rcpf(a[i][1])};
#pragma unroll
  for (int i = 0; i < N; ++i) x[i] *= a[i];
}
DI f2 lo2(f4 v) { return f2{v[0], v[1]}; }
DI f2 hi2(f4 v) { return f2{v[2], v[3]}; }

#define SB() __builtin_amdgcn_sched_barrier(0)
#define G5STB -1
#define GLSTB -1
#define FSTAMP(i) do {} while (0)
#define DRAIN() asm volatile("s_waitcnt vmcnt(0)" ::: "memory")
#define BAR() __syncthreads()
#define RAWBAR() do { SB(); asm volatile("s_waitcnt lgkmcnt(0)" ::: "memory"); __builtin_amdgcn_s_barrier(); asm volatile("" ::: "memory"); SB(); } while (0)

struct Ctx {
  rsrc_t ws;
  u64 tag;
  char* wsp;
  int lane, w, tok, g;
};

DI GAS u64* flag_ptr(const Ctx& c, int group, int idx) { return (GAS u64*)(c.wsp + OFF_FLAG + group * 256 + idx * 8); }
DI void publish(const Ctx& c, int group, int idx) {
  DRAIN();
  RAWBAR();
  if (threadIdx.x == 0) __hip_atomic_store(flag_ptr(c, group, idx), c.tag, __ATOMIC_RELAXED, __HIP_MEMORY_SCOPE_AGENT);
}
template <int NAFTER> DI void publish_n(const Ctx& c, int group, int idx) {
  asm volatile("s_waitcnt vmcnt(%0)" :: "n"(NAFTER) : "memory");
  RAWBAR();
  if (threadIdx.x == 0) __hip_atomic_store(flag_ptr(c, group, idx), c.tag, __ATOMIC_RELAXED, __HIP_MEMORY_SCOPE_AGENT);
}
DI void wait_flags(const Ctx& c, int byte_off, bool active) {
  GAS u64* p = (GAS u64*)(c.wsp + byte_off);
  GAS u64* ab = (GAS u64*)(c.wsp + OFF_ABORT);
  for (unsigned spins = 0;;) {
    u64 v = c.tag;
    if (active) v = __hip_atomic_load(p, __ATOMIC_RELAXED, __HIP_MEMORY_SCOPE_AGENT);
    if (__all(v == c.tag)) break;
    ++spins;
    if ((spins & 63u) == 0u) {
      u64 a = __hip_atomic_load(ab, __ATOMIC_RELAXED, __HIP_MEMORY_SCOPE_AGENT);
      if (__any(a == c.tag)) break;
      if (spins > SPIN_CAP) {
        __hip_atomic_store(ab, c.tag, __ATOMIC_RELAXED, __HIP_MEMORY_SCOPE_AGENT);
        break;
      }
    }
  }
  __builtin_amdgcn_fence(__ATOMIC_ACQUIRE, "wavefront");
  asm volatile("" ::: "memory");
}
DI void wait_group(const Ctx& c, int group, int first, int n) {
  wait_flags(c, OFF_FLAG + group * 256 + (first + (c.lane < n ? c.lane : 0)) * 8, c.lane < n);
}

#define STAMP(i) do {} while (0)
#define CSTAMP(i) do {} while (0)
#define SPAN(i) do {} while (0)

DI v4u gather8(const unsigned short* p, int stride) {
  unsigned short e[8];
#pragma unroll
  for (int j = 0; j < 8; ++j) e[j] = p[(size_t)j * stride];
  v4u v;
  v.x = e[0] | ((unsigned)e[1] << 16);
  v.y = e[2] | ((unsigned)e[3] << 16);
  v.z = e[4] | ((unsigned)e[5] << 16);
  v.w = e[6] | ((unsigned)e[7] << 16);
  return v;
}
DI float sum8(v4u v) { return ((blo(v.x) + bhi(v.x)) + (blo(v.y) + bhi(v.y))) + ((blo(v.z) + bhi(v.z)) + (blo(v.w) + bhi(v.w))); }

struct TaskB { const unsigned short* src; int stride; int cpr; int nfrag; unsigned dst; int kg; unsigned cs; };
DI TaskB decode_b(const Params& P, int t, bool on) {
  TaskB k;
  int kq, sl, base, csoff = -1;
  if (t < 480) {
    const int m = t / 160, tt = t - 160 * m;
    k.src = m == 0 ? P.wo : (m == 1 ? P.w1 : P.w2);
    k.stride = 640; kq = tt >> 3; sl = tt & 7; k.cpr = 10; k.nfrag = 5; k.kg = 80; base = OFF_WO + m * SZ_W6;
    if (m == 1) csoff = OFF_CS1 + (kq * 640 + 80 * sl) * 4;
  } else if (t < 800) {
    const int tt = t - 480;
    k.src = P.w3; k.stride = 1536; kq = tt >> 4; sl = tt & 15; k.cpr = 12; k.nfrag = 6; k.kg = 80; base = OFF_W3;
    csoff = OFF_CS3 + (kq * 1536 + 96 * sl) * 4;
  } else {
    const int tt = t - 800;
    k.src = P.w4; k.stride = 640; kq = tt >> 3; sl = tt & 7; k.cpr = 10; k.nfrag = 5; k.kg = 192; base = OFF_W4;
  }
  k.src += (size_t)(32 * kq) * k.stride + sl * (8 * k.cpr);
  k.dst = on ? (unsigned)(base + (sl * k.nfrag * k.kg + 4 * kq) * 256) : OOB;
  k.cs = (on && csoff >= 0) ? (unsigned)csoff : OOB;
  return k;
}
constexpr int RB_ROW = 208;
DI void task_load(const TaskB& k, int lane, v4u (&r)[6], int (&lo)[6]) {
  const int magic = k.cpr == 10 ? 6554 : 5462;
#pragma unroll
  for (int i = 0; i < 6; ++i) {
    const int ch = i * 64 + lane;
    const int row = (ch * magic) >> 16, cc = ch - row * k.cpr;
    const bool ok = row < 32;
    lo[i] = ok ? row * RB_ROW + cc * 16 : -1;
    r[i] = *(const GAS v4u*)((const GAS char*)k.src + (ok ? (unsigned)(row * (k.stride * 2) + cc * 16) : 0u));
  }
}
DI void task_store(const Ctx& c, const TaskB& k, LAS char* st, const v4u (&r)[6], const int (&lo)[6]) {
#pragma unroll
  for (int i = 0; i < 6; ++i)
    if (lo[i] >= 0) *(LAS v4u*)(st + lo[i]) = r[i];
  asm volatile("s_waitcnt lgkmcnt(0)" ::: "memory");
  const int q = c.tok >> 2, pc = c.tok & 3;
  const LAS char* r0 = st + (8 * c.g + q) * RB_ROW;
  v4u f[6];
#pragma unroll
  for (int nt = 0; nt < 6; ++nt) {
    const bool last = nt == 4 && k.cpr == 10;
    const int cb = last ? (64 + 4 * pc) * 2 : (32 * (nt >> 1) + 8 * pc + 4 * (nt & 1)) * 2;
    const LAS char* a = r0 + (nt < k.nfrag ? cb : 0);
    const s4 l4 = __builtin_amdgcn_ds_read_tr16_b64_v4i16((LAS s4*)a);
    const s4 h4 = __builtin_amdgcn_ds_read_tr16_b64_v4i16((LAS s4*)(a + 4 * RB_ROW));
    f[nt] = __builtin_bit_cast(v4u, __builtin_shufflevector(l4, h4, 0, 1, 2, 3, 4, 5, 6, 7));
  }
  const unsigned d0 = k.dst + (unsigned)(c.g * 256 + c.tok * 16);
#pragma unroll
  for (int nt = 0; nt < 6; ++nt) st16(c.ws, nt < k.nfrag ? d0 + (unsigned)(nt * k.kg * 256) : OOB, 0, f[nt]);
  if (k.cs != OOB) {
#pragma unroll
    for (int nt = 0; nt < 6; ++nt) {
      const float cs = xsum(sum8(f[nt]));
      st4(c.ws, (nt < k.nfrag && c.g == 0) ? k.cs + (unsigned)((nt * 16 + c.tok) * 4) : OOB, 0, asu(cs));
    }
  }
  asm volatile("s_waitcnt lgkmcnt(0)" ::: "memory");
}

struct G5 { unsigned voff[4]; int so[3]; unsigned red[3]; unsigned src; };
template <int KS> DI G5 gemm5_prep(const Ctx& c, int xoff, int tile, int p, int shift) {
  G5 a;
#pragma unroll
  for (int pos = 0; pos < 4; ++pos) {
    const int tt = (c.w + 1 + pos) & 3;
    const int t = 16 * (4 * p + tt) + c.tok + shift;
    a.voff[pos] = (t >= 0 && t < SEQ) ? (unsigned)((t >> 4) * tile + c.g * 256 + (t & 15) * 16) : OOB;
    if (pos < 3) a.red[pos] = L_RED + ((tt * 3 + (2 - pos)) * 5) * 1024 + c.lane * 16;
  }
  a.src = L_RED + (c.w * 15) * 1024 + c.lane * 16;
  int so = xoff + KS * c.w * 1024;
#pragma unroll
  for (int i = 0; i < 3; ++i) a.so[i] = __builtin_amdgcn_readfirstlane(so + i * 4096);
  asm volatile("" : "+v"(a.voff[0]), "+v"(a.voff[1]), "+v"(a.voff[2]), "+v"(a.voff[3]), "+v"(a.red[0]), "+v"(a.red[1]), "+v"(a.red[2]), "+v"(a.src));
  asm volatile("" : "+s"(a.so[0]), "+s"(a.so[1]), "+s"(a.so[2]));
  return a;
}
template <int KS, int D, int STB = -1, class F>
DI void gemm5(const Ctx& c0, LAS char* lds, const G5& a, const v4u (&wf)[5][KS], f4 (&out)[5], F slot) {
  const Ctx& c = c0;
  v4u xf[4 * KS];
#define G5X(k) xf[k] = ld16(c.ws, a.voff[(k) / KS] + (((k) % KS) & 3) * 1024, a.so[((k) % KS) >> 2])
#pragma unroll
  for (int k = 0; k < D; ++k) { G5X(k); SB(); }
  if (STB >= 0) STAMP(STB);
  f4 acc[2][5];
#pragma unroll
  for (int pos = 0; pos < 4; ++pos) {
#pragma unroll
    for (int nt = 0; nt < 5; ++nt) acc[pos & 1][nt] = f4{0.f, 0.f, 0.f, 0.f};
#pragma unroll
    for (int u = 0; u < KS; ++u) {
      const int k = pos * KS + u;
#pragma unroll
      for (int nt = 0; nt < 5; ++nt) acc[pos & 1][nt] = mfma(wf[nt][u], xf[k], acc[pos & 1][nt]);
      if (k + D < 4 * KS) G5X(k + D);
      slot(k);
      if (pos >= 1) {
#pragma unroll
        for (int nt = 0; nt < 5; ++nt)
          if (nt * KS / 5 == u) *(LAS f4*)(lds + a.red[pos - 1] + nt * 1024) = acc[(pos - 1) & 1][nt];
      }
      __builtin_amdgcn_sched_group_barrier(0x008, 2, 0);
      __builtin_amdgcn_sched_group_barrier(0x020, 1, 0);
      __builtin_amdgcn_sched_group_barrier(0x008, 1, 0);
      __builtin_amdgcn_sched_group_barrier(0x080, 1, 0);
      __builtin_amdgcn_sched_group_barrier(0x008, 1, 0);
      __builtin_amdgcn_sched_group_barrier(0x020, 1, 0);
      __builtin_amdgcn_sched_group_barrier(0x008, 1, 0);
      SB();
    }
    if (STB >= 0 && pos == 0) STAMP(STB + 1);
    if (STB >= 0 && pos == 2) STAMP(STB + 2);
  }
#undef G5X
  if (STB >= 0) STAMP(STB + 3);
  RAWBAR();
  f4 part[15];
#pragma unroll
  for (int e = 0; e < 15; ++e) part[e] = *(const LAS f4*)(lds + a.src + e * 1024);
  SB();
  if (STB >= 0) STAMP(STB + 4);
#pragma unroll
  for (int nt = 0; nt < 5; ++nt) out[nt] = (acc[1][nt] + part[nt]) + (part[5 + nt] + part[10 + nt]);
}

template <int NT, int NTT, int LK, int STB = -1, class FX, class FE>
DI void gemm_lds(const Ctx& c0, const LAS char* wl, f4 (&acc)[NT][NTT], FX xload, FE extra) {
  constexpr int D = 2;
  v4u xf[LK][NTT];
#pragma unroll
  for (int t = 0; t < LK; ++t)
#pragma unroll
    for (int t2 = 0; t2 < NTT; ++t2) { xf[t][t2] = xload(t, t2); SB(); }
  if (STB >= 0) STAMP(STB);
  RAWBAR();
  if (STB >= 0) STAMP(STB + 1);
  v4u wb[D + 1][NT];
#pragma unroll
  for (int d = 0; d < D; ++d)
#pragma unroll
    for (int nt = 0; nt < NT; ++nt) wb[d][nt] = *(const LAS v4u*)(wl + (nt * 20 + d) * 1024);
#pragma unroll
  for (int nt = 0; nt < NT; ++nt)
#pragma unroll
    for (int t2 = 0; t2 < NTT; ++t2) acc[nt][t2] = f4{0.f, 0.f, 0.f, 0.f};
  SB();
#pragma unroll
  for (int t = 0; t < 20; ++t) {
    {
      v4u(&xs)[NTT] = xf[t % LK];
      v4u(&wsn)[NT] = wb[t % (D + 1)];
      if constexpr (NT == 4 && NTT == 4)
        asm volatile("" : "+v"(xs[0]), "+v"(xs[1]), "+v"(xs[2]), "+v"(xs[3]), "+v"(wsn[0]), "+v"(wsn[1]), "+v"(wsn[2]), "+v"(wsn[3]));
      else if constexpr (NT == 6 && NTT == 2)
        asm volatile("" : "+v"(xs[0]), "+v"(xs[1]), "+v"(wsn[0]), "+v"(wsn[1]), "+v"(wsn[2]), "+v"(wsn[3]), "+v"(wsn[4]), "+v"(wsn[5]));
    }
    SB();
#pragma unroll
    for (int nt = 0; nt < NT; ++nt) {
      if (t + D < 20) wb[(t + D) % (D + 1)][nt] = *(const LAS v4u*)(wl + (nt * 20 + t + D) * 1024);
#pragma unroll
      for (int t2 = 0; t2 < NTT; ++t2) acc[nt][t2] = mfma(wb[t % (D + 1)][nt], xf[t % LK][t2], acc[nt][t2]);
      if (t >= 1 && t - 1 + LK < 20 && nt < NTT) xf[(t - 1) % LK][nt] = xload(t - 1 + LK, nt);
      extra(t, nt);
      __builtin_amdgcn_sched_group_barrier(0x008, NTT / 2, 0);
      __builtin_amdgcn_sched_group_barrier(0x100, 1, 0);
      __builtin_amdgcn_sched_group_barrier(0x008, NTT - NTT / 2, 0);
      __builtin_amdgcn_sched_group_barrier(0x020, 1, 0);
    }
    SB();
    if (STB >= 0 && (t + 1) % 5 == 0) STAMP(STB + 1 + (t + 1) / 5);
  }
}

DI void store5(const Ctx& c, const f4 (&v)[5], int aoff, int tt_g, int j) {
  unsigned u[10];
#pragma unroll
  for (int nt = 0; nt < 5; ++nt) {
    u[2 * nt] = pk2(v[nt][0], v[nt][1]);
    u[2 * nt + 1] = pk2(v[nt][2], v[nt][3]);
  }
  const int so = aoff + tt_g * TILE6 + 10 * j * 256;
  const unsigned vo = c.g * 256 + c.tok * 16 + so;
  st16(c.ws, vo, 0, v4u{u[0], u[1], u[2], u[3]});
  st16(c.ws, vo, 1024, v4u{u[4], u[5], u[6], u[7]});
  st8(c.ws, (c.g >> 1) * 256 + c.tok * 16 + (c.g & 1) * 8, so + 2048, v2u{u[8], u[9]});
}
DI void store_stats(const Ctx& c, const f4 (&v)[5], int soff, int tt_g, int j) {
  f2 s2 = lo2(v[0]) + hi2(v[0]), q2 = __builtin_elementwise_fma(hi2(v[0]), hi2(v[0]), lo2(v[0]) * lo2(v[0]));
#pragma unroll
  for (int nt = 1; nt < 5; ++nt) {
    s2 += lo2(v[nt]);
    s2 += hi2(v[nt]);
    q2 = __builtin_elementwise_fma(lo2(v[nt]), lo2(v[nt]), q2);
    q2 = __builtin_elementwise_fma(hi2(v[nt]), hi2(v[nt]), q2);
  }
  float s = s2[0] + s2[1], q = q2[0] + q2[1];
  xsum2(s, q);
  st8(c.ws, c.g == 0 ? (unsigned)(c.tok * 64 + j * 8) : OOB, soff + tt_g * 1024, v2u{asu(s), asu(q)});
}
DI void keep(v4u v) { asm volatile("" :: "v"(v)); }
DI void warm5(const Ctx& c, int aoff, int tt_g, int j, v4u (&wv)[3]) {
  const int so = aoff + tt_g * TILE6 + 10 * j * 256;
  wv[0] = ld16(c.ws, c.lane * 16, so);
  wv[1] = ld16(c.ws, c.lane * 16, so + 1024);
  wv[2] = ld16(c.ws, c.lane < 32 ? (unsigned)(c.lane * 16) : OOB, so + 2048);
}
DI void stats_from(v4u sv, float& mean, float& var) {
  float s = asf(sv.x) + asf(sv.z), q = asf(sv.y) + asf(sv.w);
  xsum2(s, q);
  mean = s * (1.0f / DM);
  var = __builtin_fmaxf(q * (1.0f / DM) - mean * mean, 0.f);
}

template <int NQ> DI void features(const f4 (&p0)[NQ], const f4 (&p1)[NQ], const float* lr, float inf, unsigned (&ph)[NQ][5]) {
  f2 ua[NQ], ub[NQ], uc[NQ], of[NQ];
#pragma unroll
  for (int q = 0; q < NQ; ++q) {
    const f2 l2 = f2{lr[q], lr[q]};
    ua[q] = lo2(p0[q]) * l2;
    ub[q] = hi2(p0[q]) * l2;
    uc[q] = f2{p1[q][0] * lr[q], 0.f};
    float mx = __builtin_fmaxf(__builtin_fmaxf(__builtin_fmaxf(ua[q][0], ua[q][1]), ub[q][0]), __builtin_fmaxf(ub[q][1], uc[q][0]));
    float mn = __builtin_fmaxf(__builtin_fmaxf(__builtin_fmaxf(-ua[q][0], -ua[q][1]), -ub[q][0]), __builtin_fmaxf(-ub[q][1], -uc[q][0]));
    of[q] = f2{mx, mn};
  }
#pragma unroll
  for (int q = 0; q < NQ; ++q) {
    float mx = of[q][0], mn = of[q][1];
    xmax2(mx, mn, inf);
    of[q] = f2{mx, mn};
  }
  f2 e[NQ][5];
  float sp[NQ], sn[NQ];
#pragma unroll
  for (int q = 0; q < NQ; ++q) {
    asm("v_pk_add_f32 %0, %5, %8 op_sel:[0,0] op_sel_hi:[0,1] neg_lo:[0,1] neg_hi:[1,1]\n\t"
        "v_pk_add_f32 %1, %5, %8 op_sel:[1,0] op_sel_hi:[1,1] neg_lo:[0,1] neg_hi:[1,1]\n\t"
        "v_pk_add_f32 %2, %6, %8 op_sel:[0,0] op_sel_hi:[0,1] neg_lo:[0,1] neg_hi:[1,1]\n\t"
        "v_pk_add_f32 %3, %6, %8 op_sel:[1,0] op_sel_hi:[1,1] neg_lo:[0,1] neg_hi:[1,1]\n\t"
        "v_pk_add_f32 %4, %7, %8 op_sel:[0,0] op_sel_hi:[0,1] neg_lo:[0,1] neg_hi:[1,1]"
        : "=&v"(e[q][0]), "=&v"(e[q][1]), "=&v"(e[q][2]), "=&v"(e[q][3]), "=&v"(e[q][4])
        : "v"(ua[q]), "v"(ub[q]), "v"(uc[q]), "v"(of[q]));
  }
#pragma unroll
  for (int q = 0; q < NQ; ++q) {
#pragma unroll
    for (int i = 0; i < 5; ++i) e[q][i] = f2{__builtin_amdgcn_exp2f(e[q][i][0]), __builtin_amdgcn_exp2f(e[q][i][1])};
    const f2 sm = ((e[q][0] + e[q][1]) + (e[q][2] + e[q][3])) + e[q][4];
    sp[q] = sm[0];
    sn[q] = sm[1];
  }
#pragma unroll
  for (int q = 0; q < NQ; ++q) xsum2(sp[q], sn[q]);
#pragma unroll
  for (int q = 0; q < NQ; ++q) {
    const f2 inv = f2{__builtin_amdgcn_rcpf(sp[q]), __builtin_amdgcn_rcpf(sn[q])};
#pragma unroll
    for (int i = 0; i < 5; ++i) {
      const f2 y = e[q][i] * inv;
      ph[q][i] = pk2(y[0], y[1]);
    }
  }
}

#define HK_F 2
#define MAP_P(x, i) (8 * ((x) >> 1) + ((i) >> 2))
#define MAP_J(x, i) (4 * ((x) & 1) + ((i) & 3))
#define MAP_G(x, i) (2 * ((x) >> 1) + ((i) >> 4))
#define MAP_H(x, i) (16 * ((x) & 1) + ((i) & 15))
#define MAP_Q(x, i) (4 * ((x) >> 1) + ((i) >> 3))
#define MAP_I(x, i) (8 * ((x) & 1) + ((i) & 7))
#define PHASE_IDS()                                                                     \
  Ctx c = c0;                                                                           \
  asm volatile("" : "+v"(c.lane));                                                      \
  asm volatile("" : "+s"(c.w));                                                         \
  c.tok = c.lane & 15;                                                                  \
  c.g = c.lane >> 4;                                                                    \
  int bb_ = blockIdx.x;                                                                 \
  asm volatile("" : "+s"(bb_));                                                         \
  const int xg = bb_ & 7, idx = bb_ >> 3;                                               \
  const int p = MAP_P(xg, idx), j = MAP_J(xg, idx);                                     \
  const int G = MAP_G(xg, idx), h = MAP_H(xg, idx);                                     \
  const int q6 = MAP_Q(xg, idx), i6 = MAP_I(xg, idx);                                   \
  const int w = c.w, lane = c.lane, tok = c.tok, g = c.g;                               \
  const int tid = w * 64 + lane;                                                        \
  const int ttg = 4 * p + w;                                                            \
  (void)p; (void)j; (void)G; (void)h; (void)q6; (void)i6; (void)tok; (void)g; (void)tid; (void)ttg; (void)lane;

#define WOFF(x) (x)
#define W5(arr, off, i) arr[(i) % 5][(i) / 5] = ld16(c.ws, lane * 16 + (((i) / 5) & 3) * 1024, WOFF((off) + (5 * j + (i) % 5) * 20480 + 5120 * w + (((i) / 5) >> 2) * 4096))
#define W4(e) w4f[(e) % 5][(e) / 5] = ld16(c.ws, lane * 16 + (((e) / 5) & 3) * 1024, WOFF(OFF_W4 + (5 * j + (e) % 5) * 49152 + 12288 * w + (((e) / 5) >> 2) * 4096))
#define DMA_BASES()                                                                                   \
  int w3g = WOFF(OFF_W3 + (i6 * 120 + w) * 1024), wqg = WOFF(OFF_WQ + (h * 80 + w * 20) * 1024);      \
  int w3l = L_BIG + w * 1024, wql = L_BIG + w * 20480;                                                \
  asm volatile("" : "+s"(w3g), "+s"(wqg), "+s"(w3l), "+s"(wql));     \
  (void)w3g; (void)wqg; (void)w3l; (void)wql;
#define W3DMA(i) dma16(c.ws, lds + w3l + (i) * 4096, lane * 16, w3g + (i) * 4096)
#define WQDMA(u) dma16(c.ws, lds + wql + (u) * 1024, lane * 16, wqg + (u) * 1024)
#define G5D 7
#define P1LK 5
#define P6LK 7

__global__ void __launch_bounds__(256, 1) fused_kernel(Params P) {
  extern __shared__ __attribute__((aligned(16))) char smem[];
  LAS char* lds = (LAS char*)smem;
  Ctx c0;
  c0.wsp = P.ws;
  c0.ws = __builtin_amdgcn_make_buffer_rsrc((void*)P.ws, 0, WS_USED, 0x00020000);
  c0.lane = threadIdx.x & 63;
  c0.w = __builtin_amdgcn_readfirstlane(threadIdx.x >> 6);
  c0.tok = c0.lane & 15;
  c0.g = c0.lane >> 4;
  c0.tag = 0;
  STAMP(0);
  CSTAMP(62);
  SPAN(0);
  unsigned codepf[2];
  {
    u64 pc;
    asm volatile("s_getpc_b64 %0" : "=s"(pc));
    const GAS unsigned* cp = (const GAS unsigned*)(pc & ~(u64)127);
#pragma unroll
    for (int i = 0; i < 2; ++i) {
      const int line = (i * 4 + c0.w) * 64 + c0.lane;
      codepf[i] = cp[(line < CODE_LINES ? line : 0) * 32];
    }
  }
  v4u wph[2];
  f4 x2[5], x1[5];
  {
    PHASE_IDS();
    DMA_BASES();
    const rsrc_t rx = __builtin_amdgcn_make_buffer_rsrc((void*)P.x, 0, SEQ * DM * 2, 0x00020000);
    v2u ar[5];
    int alo[5];
#pragma unroll
    for (int i = 0; i < 5; ++i) {
      const int e = tid + 256 * i, row = (e * 4370) >> 16, pc = e - 15 * row, sec = (pc * 13) >> 6;
      const bool ok = e < 1200;
      alo[i] = ok ? row * 136 + pc * 8 : -1;
      ar[i] = *(const GAS v2u*)((const GAS char*)P.wqkv + 80 * G * 3840 + 40 * h + (ok ? (unsigned)(row * 3840 + sec * 1280 + (pc - 5 * sec) * 8) : 0u));
    }
    const int tk = 256 * G + 8 * h + 2 * w + (lane >> 5);
    v4u xr[3];
#pragma unroll
    for (int q = 0; q < 3; ++q) {
      const int ch = (lane & 31) + 32 * q;
      xr[q] = ld16p(rx, ch < 80 ? (unsigned)(tk * (DM * 2) + ch * 16) : OOB, 0);
    }
    const u64 ep = __hip_atomic_load((GAS u64*)(P.ws + OFF_EP), __ATOMIC_RELAXED, __HIP_MEMORY_SCOPE_AGENT);
    SB();
    FSTAMP(40);
    {
#pragma unroll
      for (int i = 0; i < 5; ++i)
        if (alo[i] >= 0) *(LAS v2u*)(lds + L_AROW + alo[i]) = ar[i];
      float sm = 0.f, sq = 0.f;
#pragma unroll
      for (int q = 0; q < 3; ++q) {
        const int ch = (lane & 31) + 32 * q;
        if (ch < 80) *(LAS v4u*)(lds + L_XROW + (2 * w + (lane >> 5)) * 1296 + ch * 16) = xr[q];
        const unsigned e[4] = {xr[q].x, xr[q].y, xr[q].z, xr[q].w};
#pragma unroll
        for (int k = 0; k < 4; ++k) {
          const float a = blo(e[k]), b = bhi(e[k]);
          sm += a + b;
          sq += a * a + b * b;
        }
      }
      c0.tag = ((u64)__builtin_amdgcn_readfirstlane((unsigned)((ep + 1) >> 32)) << 32) | __builtin_amdgcn_readfirstlane((unsigned)(ep + 1));
      c.tag = c0.tag;
      RAWBAR();
      FSTAMP(42);
      float cs = 0.f;
      {
        const int gp = tok >> 2, v = 4 * w + (tok & 3), sec = (v * 13) >> 6;
        const bool valid = v < 15;
        const int col = valid ? 20 * sec + 5 * gp + (v - 5 * sec) : 0;
        unsigned short e[3][8];
#pragma unroll
        for (int q = 0; q < 3; ++q) {
          const int kl = 4 * q + ((q == 2) ? (g & 1) : g);
          const LAS unsigned short* src = (const LAS unsigned short*)(lds + L_AROW + 8 * kl * 136) + col;
#pragma unroll
          for (int jj = 0; jj < 8; ++jj) e[q][jj] = src[jj * 68];
        }
        v4u xv[3];
#pragma unroll
        for (int q = 0; q < 3; ++q) {
          const int e2 = tid + 256 * q, grp = e2 >> 3, tk8 = e2 & 7;
          xv[q] = *(const LAS v4u*)(lds + L_XROW + tk8 * 1296 + (e2 < 640 ? grp : 0) * 16);
        }
        SB();
#pragma unroll
        for (int q = 0; q < 3; ++q) {
          const int e2 = tid + 256 * q, grp = e2 >> 3, tk8 = e2 & 7;
          st16(c.ws, e2 < 640 ? (unsigned)((16 * G + (h >> 1)) * TILE6 + grp * 256 + (8 * (h & 1) + tk8) * 16) : OOB, OFF_ACT + A_X3, xv[q]);
        }
#pragma unroll
        for (int q = 0; q < 3; ++q) {
          v4u f = v4u{e[q][0] | ((unsigned)e[q][1] << 16), e[q][2] | ((unsigned)e[q][3] << 16), e[q][4] | ((unsigned)e[q][5] << 16), e[q][6] | ((unsigned)e[q][7] << 16)};
          if (!valid) f = v4u{0u, 0u, 0u, 0u};
          const bool on = (q < 2) || (g < 2);
          st16(c.ws, on ? (unsigned)(((h * 4 + w) * 80 + 10 * G + 4 * q + g) * 256 + tok * 16) : OOB, OFF_WQ, f);
          cs += on ? sum8(f) : 0.f;
        }
      }
      cs = xsum(cs);
      st4(c.ws, g == 0 ? (unsigned)(((h * 8 + G) * 64 + w * 16 + tok) * 4) : OOB, OFF_CSQ, asu(cs));
#pragma unroll
      for (int o = 1; o < 32; o <<= 1) {
        sm += __shfl_xor(sm, o);
        sq += __shfl_xor(sq, o);
      }
      st16(c.ws, (lane & 31) < 4 ? (unsigned)(tk * 64 + (lane & 31) * 16) : OOB, OFF_ACT + A_S3,
           (lane & 31) == 0 ? v4u{asu(sm), asu(sq), 0u, 0u} : v4u{0u, 0u, 0u, 0u});
      FSTAMP(43);
      DRAIN();
      RAWBAR();
      if (tid == 0) {
        __hip_atomic_store(flag_ptr(c, FG_A1 + h, G), c.tag, __ATOMIC_RELAXED, __HIP_MEMORY_SCOPE_AGENT);
        __hip_atomic_store(flag_ptr(c, FG_A2 + G * 4 + (h >> 3), h & 7), c.tag, __ATOMIC_RELAXED, __HIP_MEMORY_SCOPE_AGENT);
      }
    }
    STAMP(1);
    asm volatile("" :: "v"(codepf[0]), "v"(codepf[1]));
    const int wi = (xg * 32 + idx) * 4 + w;
    const bool two = wi % 6 == 0 && wi / 6 < 160;
    const TaskB k0 = decode_b(P, wi, true), k1 = decode_b(P, two ? 1024 + wi / 6 : wi, two);
    v4u rb0[6], rb1[6];
    int lo0[6], lo1[6];
    task_load(k0, lane, rb0, lo0);
    if (two) task_load(k1, lane, rb1, lo1);
    unsigned short we[2][5];
    {
      const int gp = tok >> 2, i = tok & 3;
#pragma unroll
      for (int R = 0; R < 2; ++R) {
        const int f = R == 0 ? 5 * gp + i : 5 * gp + 4;
#pragma unroll
        for (int jj = 0; jj < 5; ++jj) we[R][jj] = P.wphi[(h * 20 + 5 * g + jj) * 20 + f];
      }
    }
    const unsigned xvo = (unsigned)((16 * ttg + tok) * (DM * 2) + 160 * j);
    const v4u xa = ld16p(rx, xvo + g * 16, 0), xb = ld16p(rx, xvo + 64 + g * 16, 0);
    const v2u xe = ld8p(rx, xvo + 128 + g * 8, 0);
    SB();
    wait_flags(c, OFF_FLAG + (lane < 8 ? (FG_A1 + h) * 256 + lane * 8 : (FG_A2 + G * 4 + (((lane - 8) & 31) >> 3)) * 256 + ((lane - 8) & 7) * 8), lane < 40);
    FSTAMP(60);
#pragma unroll
    for (int u = 0; u < 20; ++u) WQDMA(u);
    unsigned cp[8];
#pragma unroll
    for (int gg = 0; gg < 8; ++gg) cp[gg] = ld4(c.ws, lane * 4, OFF_CSQ + (h * 8 + gg) * 256);
    SB();
    task_store(c, k0, lds + L_BROW + w * 6656, rb0, lo0);
    if (two) task_store(c, k1, lds + L_BROW + w * 6656, rb1, lo1);
    {
      float cs = 0.f;
#pragma unroll
      for (int gg = 0; gg < 8; ++gg) cs += asf(cp[gg]);
      if (w == 0) *(LAS float*)(lds + L_CSQ + lane * 4) = cs;
      const int i = tok & 3;
#pragma unroll
      for (int R = 0; R < 2; ++R) {
        const bool rv = (R == 0) || (i == 0);
        wph[R] = rv ? v4u{we[R][0] | ((unsigned)we[R][1] << 16), we[R][2] | ((unsigned)we[R][3] << 16), (unsigned)we[R][4], 0u} : v4u{0u, 0u, 0u, 0u};
      }
      x2[0] = f4{blo(xa.x), bhi(xa.x), blo(xa.y), bhi(xa.y)};
      x2[1] = f4{blo(xa.z), bhi(xa.z), blo(xa.w), bhi(xa.w)};
      x2[2] = f4{blo(xb.x), bhi(xb.x), blo(xb.y), bhi(xb.y)};
      x2[3] = f4{blo(xb.z), bhi(xb.z), blo(xb.w), bhi(xb.w)};
      x2[4] = f4{blo(xe.x), bhi(xe.x), blo(xe.y), bhi(xe.y)};
    }
    DRAIN();
    RAWBAR();
    if (tid == 0) __hip_atomic_store((GAS u64*)(P.ws + OFF_FB) + blockIdx.x, c.tag, __ATOMIC_RELAXED, __HIP_MEMORY_SCOPE_AGENT);
    STAMP(2);
  }

  v4u wa[5][5];
#pragma unroll
  for (int i = 0; i < 12; ++i) wa[i % 5][i / 5] = v4u{0u, 0u, 0u, 0u};

#pragma unroll 1
  for (int L = 0; L < 2; ++L) {
    const int act = OFF_ACT + L * LAYER_STRIDE;
    const int fgl = FG_L + L * FG_PER_L;
    v4u wb[5][5], wv[6];
    v4u phq0[4];
    unsigned phq1[4];
    {
      PHASE_IDS();
      DMA_BASES();
      const int tt0 = 16 * G + 4 * w;
      const unsigned xv = lane * 16 + tt0 * TILE6;
      if (L == 1) wait_group(c, FG_L + FG_X3 + 4 * G + w, 0, 8);
      else DRAIN();
      STAMP(3 + 20 * L);
      v4u stv[4];
      f4 acc4[4][4];
      gemm_lds<4, 4, P1LK, GLSTB>(c, lds + L_BIG + lane * 16, acc4,
                           [&](int t, int t2) { return ld16(c.ws, xv + t2 * TILE6 + (t & 3) * 1024, OFF_ACT + A_X3 + (t >> 2) * 4096); },
                           [&](int t, int nt) {
                             constexpr int T0 = 21 - P1LK;
                             if (t == T0) stv[nt] = ld16(c.ws, tok * 64 + g * 16, OFF_ACT + A_S3 + (tt0 + nt) * 1024);
                           });
      STAMP(4 + 20 * L);
      if (L == 0) {
        GAS u64* ab = (GAS u64*)(c.wsp + OFF_ABORT);
        for (unsigned spins = 0;;) {
          const v4u a = ld16(c.ws, lane * 32, OFF_FB), bq = ld16(c.ws, lane * 32 + 16, OFF_FB);
          const unsigned lo = (unsigned)c.tag, hi = (unsigned)(c.tag >> 32);
          const bool ok = a.x == lo && a.y == hi && a.z == lo && a.w == hi && bq.x == lo && bq.y == hi && bq.z == lo && bq.w == hi;
          if (__all(ok)) break;
          ++spins;
          if ((spins & 63u) == 0u) {
            u64 av = __hip_atomic_load(ab, __ATOMIC_RELAXED, __HIP_MEMORY_SCOPE_AGENT);
            if (__any(av == c.tag)) break;
            if (spins > SPIN_CAP) { __hip_atomic_store(ab, c.tag, __ATOMIC_RELAXED, __HIP_MEMORY_SCOPE_AGENT); break; }
          }
        }
        __builtin_amdgcn_fence(__ATOMIC_ACQUIRE, "wavefront");
        asm volatile("" ::: "memory");
#pragma unroll
        for (int i = 0; i < 12; ++i) W5(wa, OFF_WO, i);
      }
      float mean[4], lr[4], rstd[4];
#pragma unroll
      for (int tt = 0; tt < 4; ++tt) {
        float var;
        stats_from(stv[tt], mean[tt], var);
        rstd[tt] = __builtin_amdgcn_rsqf(var + LN_EPS);
        lr[tt] = rstd[tt] * LOG2E;
      }
      f4 csq[4];
#pragma unroll
      for (int nt = 0; nt < 4; ++nt) csq[nt] = *(const LAS f4*)(lds + L_CSQ + (nt * 16 + 4 * g) * 4);
      float inf = __builtin_inff();
      asm volatile("" : "+v"(inf));
      const f4 z4 = f4{0.f, 0.f, 0.f, 0.f};
      {
        f4 pk0[4], pk1[4];
        unsigned vb[4][3];
#pragma unroll
        for (int tt = 0; tt < 4; ++tt) {
          const f2 m2 = f2{-mean[tt], -mean[tt]}, r2 = f2{rstd[tt], rstd[tt]};

          const f2 v45 = __builtin_elementwise_fma(m2, lo2(csq[1]), lo2(acc4[1][tt]));
          const f2 v67 = __builtin_elementwise_fma(m2, hi2(csq[1]), hi2(acc4[1][tt]));
          const f2 v89 = __builtin_elementwise_fma(m2, lo2(csq[2]), lo2(acc4[2][tt]));
          const f2 vab = __builtin_elementwise_fma(m2, hi2(csq[2]), hi2(acc4[2][tt])) * r2;
          const f2 vcd = __builtin_elementwise_fma(m2, lo2(csq[3]), lo2(acc4[3][tt])) * r2;
          const float ve = __builtin_fmaf(-mean[tt], csq[3][2], acc4[3][tt][2]) * rstd[tt];
          const v4u bq = v4u{pk2(v45[1], v67[0]), pk2(v67[1], v89[0]), pk2(v89[1], 0.f), 0u};
          pk0[tt] = mfma(wph[0], bq, z4);
          pk1[tt] = mfma(wph[1], bq, z4);
          vb[tt][0] = pk2(vab[0], vab[1]);
          vb[tt][1] = pk2(vcd[0], vcd[1]);
          vb[tt][2] = pk2(ve, 1.0f);
        }
        unsigned ph[4][5];
#pragma unroll
        for (int i = 12; i < 25; ++i) W5(wa, OFF_WO, i);
        features<4>(pk0, pk1, lr, inf, ph);
#pragma unroll
        for (int i = 0; i < 13; ++i) {
          __builtin_amdgcn_sched_group_barrier(0x002, 14, 0);
          __builtin_amdgcn_sched_group_barrier(0x020, 1, 0);
        }
        SB();
#pragma unroll
        for (int tt = 0; tt < 4; ++tt) {
          LAS char* row = lds + L_SROW + (64 * w + 16 * tt + tok) * SROW;
          *(LAS v2u*)(row + g * 24) = v2u{ph[tt][0], ph[tt][1]};
          *(LAS v2u*)(row + g * 24 + 8) = v2u{ph[tt][2], ph[tt][3]};
          *(LAS v2u*)(row + g * 24 + 16) = v2u{ph[tt][4], 0u};
          *(LAS v2u*)(row + 96 + g * 16) = v2u{vb[tt][0], vb[tt][1]};
          *(LAS v2u*)(row + 96 + g * 16 + 8) = v2u{vb[tt][2], 0u};
        }
      }
      FSTAMP(58);
      RAWBAR();
      {
        f4 kv[3][2];
        const int qq = tok >> 2, pp = tok & 3;
        v4u fa[2][3], fb[2][2];
#pragma unroll
        for (int ks = 0; ks < 2; ++ks) {
          const LAS char* r0 = lds + L_SROW + (64 * w + 32 * ks + 8 * g + qq) * SROW + pp * 8;
#pragma unroll
          for (int a = 0; a < 3; ++a) {
            const s4 lo = __builtin_amdgcn_ds_read_tr16_b64_v4i16((LAS s4*)(r0 + a * 32));
            const s4 hi = __builtin_amdgcn_ds_read_tr16_b64_v4i16((LAS s4*)(r0 + 4 * SROW + a * 32));
            fa[ks][a] = __builtin_bit_cast(v4u, __builtin_shufflevector(lo, hi, 0, 1, 2, 3, 4, 5, 6, 7));
          }
#pragma unroll
          for (int bt = 0; bt < 2; ++bt) {
            const s4 lo = __builtin_amdgcn_ds_read_tr16_b64_v4i16((LAS s4*)(r0 + 96 + bt * 32));
            const s4 hi = __builtin_amdgcn_ds_read_tr16_b64_v4i16((LAS s4*)(r0 + 4 * SROW + 96 + bt * 32));
            fb[ks][bt] = __builtin_bit_cast(v4u, __builtin_shufflevector(lo, hi, 0, 1, 2, 3, 4, 5, 6, 7));
          }
        }
        SB();
#pragma unroll
        for (int a = 0; a < 3; ++a)
#pragma unroll
          for (int bt = 0; bt < 2; ++bt) {
            kv[a][bt] = mfma(fa[0][a], fb[0][bt], z4);
            kv[a][bt] = mfma(fa[1][a], fb[1][bt], kv[a][bt]);
          }
#pragma unroll
        for (int a = 0; a < 3; ++a)
#pragma unroll
          for (int bt = 0; bt < 2; ++bt) *(LAS f4*)(lds + L_KVW + (w * 6 + a * 2 + bt) * 1024 + lane * 16) = kv[a][bt];
      }
      RAWBAR();
      {
        const int e1 = 256 + (tid & 127);
        f4 t0[4], t1[4];
#pragma unroll
        for (int ww = 0; ww < 4; ++ww) {
          t0[ww] = *(const LAS f4*)(lds + L_KVW + ww * 6144 + tid * 16);
          t1[ww] = *(const LAS f4*)(lds + L_KVW + ww * 6144 + e1 * 16);
        }
        SB();
        FSTAMP(59);
        const int ko = OFF_KVP + L * SZ_KVP + (h * 8 + G) * 6144;
        st16(c.ws, tid * 16, ko, asu4((t0[0] + t0[1]) + (t0[2] + t0[3])));
        st16(c.ws, tid < 128 ? (unsigned)(e1 * 16) : OOB, ko, asu4((t1[0] + t1[1]) + (t1[2] + t1[3])));
      }
      SB();
#pragma unroll
      for (int i = 0; i < 12; ++i) W5(wb, OFF_W1, i);
      SB();
      publish_n<12>(c, fgl + FG_KV + h, G);
      STAMP(5 + 20 * L);
      if (L == 0) {
        const int t = tid & 127;
        const bool three = w >= 2;
        const bool on = t < (three ? 96 : 80);
        const unsigned vo = on ? (unsigned)(((three ? 96 * i6 : 80 * j) + t) * 4) : OOB;
        const int so = three ? OFF_CS3 : OFF_CS1, st = three ? 1536 * 4 : 640 * 4;
        unsigned cp[20];
#pragma unroll
        for (int kq = 0; kq < 20; ++kq) cp[kq] = ld4(c.ws, vo, so + kq * st);
        SB();
        float cs = 0.f;
#pragma unroll
        for (int kq = 0; kq < 20; ++kq) cs += asf(cp[kq]);
        if (on) *(LAS float*)(lds + (three ? L_CS3 : L_CS1) + t * 4) = cs;
      }
      SB();
#pragma unroll
      for (int i = 12; i < 25; ++i) W5(wb, OFF_W1, i);
      {
        f4 pq0[4], pq1[4];
#pragma unroll
        for (int tt = 0; tt < 4; ++tt) {
          const f2 m2 = f2{-mean[tt], -mean[tt]};

          const f2 v01 = __builtin_elementwise_fma(m2, lo2(csq[0]), lo2(acc4[0][tt]));
          const f2 v23 = __builtin_elementwise_fma(m2, hi2(csq[0]), hi2(acc4[0][tt]));
          const float v4 = __builtin_fmaf(-mean[tt], csq[1][0], acc4[1][tt][0]);
          const v4u bq = v4u{pk2(v01[0], v01[1]), pk2(v23[0], v23[1]), pk2(v4, 0.f), 0u};
          pq0[tt] = mfma(wph[0], bq, z4);
          pq1[tt] = mfma(wph[1], bq, z4);
        }
        unsigned ph[4][5];
        features<4>(pq0, pq1, lr, inf, ph);
#pragma unroll
        for (int tt = 0; tt < 4; ++tt) {
          phq0[tt] = v4u{ph[tt][0], ph[tt][1], ph[tt][2], ph[tt][3]};
          phq1[tt] = ph[tt][4];
        }
#pragma unroll
        for (int i = 0; i < 13; ++i) {
          __builtin_amdgcn_sched_group_barrier(0x002, 12, 0);
          __builtin_amdgcn_sched_group_barrier(0x020, 1, 0);
        }
        SB();
#pragma unroll
        for (int tt = 0; tt < 4; ++tt) asm volatile("" : "+v"(phq0[tt]), "+v"(phq1[tt]));
      }
    }
    {
      PHASE_IDS();
      DMA_BASES();
      const bool odd = h & 1;
      const int e1 = 256 + (tid & 127);
      unsigned kbw[2], kbr[3];
#pragma unroll
      for (int rep = 0; rep < 2; ++rep) {
        const int e = rep ? e1 : tid, tl = e >> 6, ln = e & 63;
        const int pr = 8 * (tl >> 1) + 2 * (ln >> 4);
        kbw[rep] = L_KB + (16 * (tl & 1) + (ln & 15)) * 144 + (pr / 6) * 32 + (pr % 6) * 4;
      }
      {
        const int gp = tok >> 2, i = tok & 3;
#pragma unroll
        for (int R = 0; R < 3; ++R) {
          int bs = 5;
          if (R < 2) {
            const int d = odd ? (gp == 0 ? i : 8 * gp - 4 + 4 * R + i) : 8 * gp + 4 * R + i;
            const bool rv = odd ? (gp == 0 ? R == 0 : gp < 3) : d < 20;
            bs = rv ? 8 * (d / 5) + d % 5 : 6;
          }
          kbr[R] = L_KB + bs * 144 + g * 32;
        }
      }
      const bool full = odd ? (g == 1 || g == 2) : (g < 2);
      const bool half = odd ? (g == 0) : (g == 2);
      const int so0 = act + A_O + (16 * G + 4 * w) * TILE6 + ((20 * h) >> 3) * 256;
      const unsigned vfull = full ? (unsigned)(g * 256 + tok * 16 + so0) : OOB, vhalf = half ? (unsigned)(g * 256 + tok * 16 + (odd ? 8 : 0) + so0) : OOB;
      const unsigned v1 = tid < 128 ? (unsigned)(e1 * 16) : OOB;
      asm volatile("" :: "v"(kbw[0]), "v"(kbw[1]), "v"(kbr[0]), "v"(kbr[1]), "v"(kbr[2]), "v"(vfull), "v"(vhalf), "v"(v1));
      wait_group(c, fgl + FG_KV + h, 0, 8);
      STAMP(6 + 20 * L);
      {
        const int ko = OFF_KVP + L * SZ_KVP + h * 8 * 6144;
        v4u t0[8], t1[8];
#pragma unroll
        for (int gg = 0; gg < 8; ++gg) {
          t0[gg] = ld16(c.ws, tid * 16, ko + gg * 6144);
          t1[gg] = ld16(c.ws, v1, ko + gg * 6144);
        }
        SB();
#pragma unroll
        for (int rep = 0; rep < 2; ++rep) {
          const v4u* t = rep ? t1 : t0;
          const f4 s = ((asf4(t[0]) + asf4(t[1])) + (asf4(t[2]) + asf4(t[3]))) + ((asf4(t[4]) + asf4(t[5])) + (asf4(t[6]) + asf4(t[7])));
          if (rep == 0 || tid < 128) *(LAS v2u*)(lds + kbw[rep]) = v2u{pk2(s[0], s[1]), pk2(s[2], s[3])};
        }
      }
      RAWBAR();
      v4u ka0[3];
      unsigned ka1[3];
#pragma unroll
      for (int R = 0; R < 3; ++R) {
        ka0[R] = *(const LAS v4u*)(lds + kbr[R]);
        ka1[R] = *(const LAS unsigned*)(lds + kbr[R] + 16);
      }
      f4 o[4][3];
#pragma unroll
      for (int tt = 0; tt < 4; ++tt)
#pragma unroll
        for (int R = 0; R < 3; ++R) o[tt][R] = mfma(ka0[R], phq0[tt], f4{0.f, 0.f, 0.f, 0.f});
#pragma unroll
      for (int tt = 0; tt < 4; ++tt)
#pragma unroll
        for (int R = 0; R < 3; ++R) o[tt][R] = mfma(v4u{ka1[R], 0u, 0u, 0u}, v4u{phq1[tt], 0u, 0u, 0u}, o[tt][R]);
#pragma unroll
      for (int tt = 0; tt < 4; ++tt) {
        const float inv = __builtin_amdgcn_rcpf(o[tt][2][0] + 1e-6f);
        const f2 i2 = f2{inv, inv};
        const f2 a = lo2(o[tt][0]) * i2, b = hi2(o[tt][0]) * i2, d = lo2(o[tt][1]) * i2, e = hi2(o[tt][1]) * i2;
        const unsigned u0 = pk2(a[0], a[1]), u1 = pk2(b[0], b[1]), u2 = pk2(d[0], d[1]), u3 = pk2(e[0], e[1]);
        st16(c.ws, vfull, tt * TILE6, v4u{u0, u1, u2, u3});
        st8(c.ws, vhalf, tt * TILE6, v2u{u0, u1});
      }
      publish(c, fgl + FG_O + G * 4 + (h >> 3), h & 7);
#pragma unroll
      for (int e = 0; e < 2; ++e) wv[e] = ld16(c.ws, tok * 16 + g * TILE6 + (e + (odd ? 1 : 0)) * 256, so0);
      wv[2] = wv[0];
      STAMP(7 + 20 * L);
#pragma unroll
      for (int i = 0; i < 12; ++i) W3DMA(i);
    }
    {
      PHASE_IDS();
      DMA_BASES();
      const G5 ga = gemm5_prep<5>(c, act + A_O, TILE6, p, 0);
      wait_group(c, fgl + FG_O + (p >> 2) * 4 + w, 0, 8);
      keep(wv[0]); keep(wv[1]); keep(wv[2]);
      STAMP(8 + 20 * L);
      f4 o[5];
      gemm5<5, G5D, G5STB>(c, lds, ga, wa, o, [&](int k) {
        if (k >= 8) W3DMA(k + 4);
      });
#pragma unroll
      for (int nt = 0; nt < 5; ++nt) x2[nt] += o[nt];
      store5(c, x2, act + A_X2, ttg, j);
      store_stats(c, x2, act + A_S2, ttg, j);
      SB();
      FSTAMP(49);
#pragma unroll
      for (int i = 0; i < 12; ++i) W5(wa, OFF_W2, i);
      SB();
      publish_n<12>(c, fgl + FG_X2 + p, j);
      { v4u t_[3]; warm5(c, act + A_X2, ttg, j, t_); wv[0] = t_[0]; wv[1] = t_[1]; wv[2] = t_[2]; }
      STAMP(9 + 20 * L);
    }
    v4u w4f[5][12];
    float m2, r2;
    {
      PHASE_IDS();
      DMA_BASES();
      const GeluK gk = gelu_consts();
      const G5 ga = gemm5_prep<5>(c, act + A_X2, TILE6, p, 0);
      wait_group(c, fgl + FG_X2 + p, 0, 8);
      keep(wv[0]); keep(wv[1]); keep(wv[2]);
      STAMP(10 + 20 * L);
      f4 o[5];
      v4u sv;
      gemm5<5, G5D>(c, lds, ga, wb, o, [&](int k) {
        if (k == 0) sv = ld16(c.ws, tok * 64 + g * 16, act + A_S2 + ttg * 1024);
        if (k >= 7) W5(wa, OFF_W2, k + 5);
      });
      {
        float var;
        stats_from(sv, m2, var);
        r2 = __builtin_amdgcn_rsqf(var + LN_EPS);
        const float rr = r2 * __builtin_amdgcn_rsqf(var * r2 * r2 + LN_EPS);
        const f2 mm = f2{-m2, -m2}, r2v = f2{rr, rr};
        f2 y[10];
#pragma unroll
        for (int nt = 0; nt < 5; ++nt) {
          const f4 cs = *(const LAS f4*)(lds + L_CS1 + (nt * 16 + 4 * g) * 4);
          y[2 * nt] = __builtin_elementwise_fma(mm, lo2(cs), lo2(o[nt])) * r2v;
          y[2 * nt + 1] = __builtin_elementwise_fma(mm, hi2(cs), hi2(o[nt])) * r2v;
        }
        gelu_n<10>(y, gk);
#pragma unroll
        for (int nt = 0; nt < 5; ++nt) o[nt] = f4{y[2 * nt][0], y[2 * nt][1], y[2 * nt + 1][0], y[2 * nt + 1][1]};
      }
      store5(c, o, act + A_H, ttg, j);
      SB();
#pragma unroll
      for (int e = 0; e < 12; ++e) W4(e);
      SB();
      publish_n<12>(c, fgl + FG_H + p, j);
      { v4u t_[3]; warm5(c, act + A_H, ttg, j, t_); wv[0] = t_[0]; wv[1] = t_[1]; wv[2] = t_[2]; }
      STAMP(11 + 20 * L);
    }
    {
      PHASE_IDS();
      DMA_BASES();
      const int shift = w == 0 ? -1 : (w == 1 ? 1 : (w == 2 ? -2 : 2));
      const int pn = p + (shift < 0 ? -1 : 1);
      const bool nv = pn >= 0 && pn < 32;
      {
        const f2 mm = f2{-m2, -m2}, rv = f2{r2, r2};
#pragma unroll
        for (int nt = 0; nt < 5; ++nt) {
          const f2 a = (lo2(x2[nt]) + mm) * rv, b = (hi2(x2[nt]) + mm) * rv;
          x1[nt] = f4{a[0], a[1], b[0], b[1]};
        }
      }
      const G5 ga = gemm5_prep<5>(c, act + A_H, TILE6, p, shift);
      wait_flags(c, OFF_FLAG + (fgl + FG_H + ((lane & 2) && nv ? pn : p)) * 256 + (2 * w + (lane & 1)) * 8, lane < 4);
      keep(wv[0]); keep(wv[1]); keep(wv[2]);
      STAMP(12 + 20 * L);
      f4 o[5];
      gemm5<5, G5D>(c, lds, ga, wa, o, [&](int k) {
        if (k >= 8) W4(k + 4);
      });
#pragma unroll
      for (int nt = 0; nt < 5; ++nt) x1[nt] += o[nt];
      store5(c, x1, act + A_X1, ttg, j);
      store_stats(c, x1, act + A_S1, ttg, j);
      SB();
#pragma unroll
      for (int e = 24; e < 30; ++e) W4(e);
      SB();
      publish_n<6>(c, fgl + FG_X1 + p, j);
      { v4u t_[3]; warm5(c, act + A_X1, ttg, j, t_); wv[0] = t_[0]; wv[1] = t_[1]; wv[2] = t_[2]; }
      STAMP(13 + 20 * L);
#pragma unroll
      for (int i = 24; i < 30; ++i) W3DMA(i);
    }
    {
      PHASE_IDS();
      DMA_BASES();
      const GeluK gk = gelu_consts();
      const int tt0 = 8 * q6 + 2 * w;
      const unsigned xv = lane * 16 + tt0 * TILE6;
      wait_group(c, fgl + FG_X1 + 2 * q6 + (w >> 1), 0, 8);
      keep(wv[0]); keep(wv[1]); keep(wv[2]);
      STAMP(14 + 20 * L);
      v4u sv[2];
      f4 acc[6][2];
      gemm_lds<6, 2, P6LK>(c, lds + L_BIG + lane * 16, acc,
                           [&](int t, int t2) { return ld16(c.ws, xv + t2 * TILE6 + (t & 3) * 1024, act + A_X1 + (t >> 2) * 4096); },
                           [&](int t, int nt) {
                             constexpr int T0 = 21 - P6LK;
                             if (t < T0) { if (nt == 3) W4(30 + t); }
                             else {
                               if (t == T0 && nt < 2) sv[nt] = ld16(c.ws, tok * 64 + g * 16, act + A_S1 + (tt0 + nt) * 1024);
                               const int e = 30 + T0 + 4 * (t - T0) + (nt - 2);
                               if (nt >= 2 && e < 60) W4(e);
                             }
                           });
#pragma unroll
      for (int e = 30 + (21 - P6LK) + 4 * (P6LK - 1); e < 60; ++e) W4(e);
#pragma unroll
      for (int t2 = 0; t2 < 2; ++t2) {
        float mean, var;
        stats_from(sv[t2], mean, var);
        const float rstd = __builtin_amdgcn_rsqf(var + LN_EPS);
        const f2 mm = f2{-mean, -mean}, rv = f2{rstd, rstd};
#pragma unroll
        for (int a = 0; a < 3; ++a) {
          f2 y[4];
#pragma unroll
          for (int hf = 0; hf < 2; ++hf) {
            const int nt = 2 * a + hf;
            const f4 cs = *(const LAS f4*)(lds + L_CS3 + (nt * 16 + 4 * g) * 4);
            y[2 * hf] = __builtin_elementwise_fma(mm, lo2(cs), lo2(acc[nt][t2])) * rv;
            y[2 * hf + 1] = __builtin_elementwise_fma(mm, hi2(cs), hi2(acc[nt][t2])) * rv;
          }
          gelu_n<4>(y, gk);
          st16(c.ws, g * 256 + tok * 16, act + A_H2 + (tt0 + t2) * TILEF + (12 * i6 + 4 * a) * 256,
               v4u{pk2(y[0][0], y[0][1]), pk2(y[1][0], y[1][1]), pk2(y[2][0], y[2][1]), pk2(y[3][0], y[3][1])});
          SB();
        }
      }
      SB();
      publish(c, fgl + FG_H2 + q6 * 2 + (i6 >> 3), i6 & 7);
#pragma unroll
      for (int e = 0; e < 6; ++e) wv[e] = ld16(c.ws, lane * 16, act + A_H2 + (tt0 + e / 3) * TILEF + (12 * i6 + 4 * (e % 3)) * 256);
      STAMP(15 + 20 * L);
      if (L == 0) {
#pragma unroll
        for (int u = 0; u < 12; ++u) WQDMA(u);
      }
    }
    {
      PHASE_IDS();
      DMA_BASES();
#pragma unroll
      for (int nt = 0; nt < 5; ++nt) x2[nt] += x1[nt];
      const G5 ga = gemm5_prep<12>(c, act + A_H2, TILEF, p, 0);
      wait_group(c, fgl + FG_H2 + (p >> 1) * 2 + (w >> 1), 4 * (w & 1), 4);
      keep(wv[0]); keep(wv[1]); keep(wv[2]); keep(wv[3]); keep(wv[4]); keep(wv[5]);
      STAMP(16 + 20 * L);
      const unsigned tqv = L == 0 ? (unsigned)(lane * 16) : OOB;
      f4 o[5];
      gemm5<12, G5D + 2>(c, lds, ga, w4f, o, [&](int k) {
        if (k >= 38 && k < 46) dma16(c.ws, lds + wql + (k - 26) * 1024, tqv, wqg + (k - 26) * 1024);
      });
#pragma unroll
      for (int nt = 0; nt < 5; ++nt) x2[nt] += o[nt];
      if (L == 0) {
        store5(c, x2, OFF_ACT + A_X3, ttg, j);
        store_stats(c, x2, OFF_ACT + A_S3, ttg, j);
        SB();
#pragma unroll
        for (int i = 0; i < 12; ++i) W5(wa, OFF_WO, i);
        SB();
        publish_n<12>(c, fgl + FG_X3 + p, j);
        { v4u t_[3]; warm5(c, OFF_ACT + A_X3, ttg, j, t_); DRAIN(); keep(t_[0]); keep(t_[1]); keep(t_[2]); }
        STAMP(17 + 20 * L);
      } else {
        const rsrc_t ro = __builtin_amdgcn_make_buffer_rsrc((void*)P.out, 0, SEQ * DM * 2, 0x00020000);
        const unsigned vo = (unsigned)((16 * ttg + tok) * (DM * 2) + 160 * j);
        __builtin_amdgcn_raw_buffer_store_b128(v4u{pk2(x2[0][0], x2[0][1]), pk2(x2[0][2], x2[0][3]), pk2(x2[1][0], x2[1][1]), pk2(x2[1][2], x2[1][3])},
                                               ro, vo + g * 16, 0, 0);
        __builtin_amdgcn_raw_buffer_store_b128(v4u{pk2(x2[2][0], x2[2][1]), pk2(x2[2][2], x2[2][3]), pk2(x2[3][0], x2[3][1]), pk2(x2[3][2], x2[3][3])},
                                               ro, vo + 64 + g * 16, 0, 0);
        __builtin_amdgcn_raw_buffer_store_b64(v2u{pk2(x2[4][0], x2[4][1]), pk2(x2[4][2], x2[4][3])}, ro, vo + 128 + g * 8, 0, 0);
        STAMP(17 + 20 * L);
      }
    }
  }
  CSTAMP(63);
  SPAN(1);
  if (blockIdx.x == 0 && threadIdx.x == 0) __hip_atomic_store((GAS u64*)(P.ws + OFF_EP), c0.tag, __ATOMIC_RELAXED, __HIP_MEMORY_SCOPE_AGENT);
}

}

extern "C" void kernel_launch(void* const* d_in, const int* in_sizes, int n_in, void* d_out, int out_size, void* d_ws, size_t ws_size,
                              hipStream_t stream) {
  (void)in_sizes; (void)n_in; (void)out_size; (void)ws_size;
  hk::Params P;
  P.x = (const unsigned short*)d_in[0];
  P.wqkv = (const unsigned short*)d_in[1];
  P.wphi = (const unsigned short*)d_in[2];
  P.wo = (const unsigned short*)d_in[3];
  P.w1 = (const unsigned short*)d_in[5];
  P.w2 = (const unsigned short*)d_in[6];
  P.w3 = (const unsigned short*)d_in[7];
  P.w4 = (const unsigned short*)d_in[8];
  P.out = (unsigned short*)d_out;
  P.ws = (char*)d_ws;
  (void)hipFuncSetAttribute((const void*)hk::fused_kernel, hipFuncAttributeMaxDynamicSharedMemorySize, hk::LDS_BYTES);
  hk::fused_kernel<<<256, 256, hk::LDS_BYTES, stream>>>(P);
}
```

```cpp
#include <hip/hip_runtime.h>
#include <stdint.h>

namespace hk {

#define DI __device__ __forceinline__
#define GAS __attribute__((address_space(1)))
#define LAS __attribute__((address_space(3)))
typedef unsigned v4u __attribute__((ext_vector_type(4)));
typedef unsigned v2u __attribute__((ext_vector_type(2)));
typedef float f4 __attribute__((ext_vector_type(4)));
typedef float f2 __attribute__((ext_vector_type(2)));
typedef short s8 __attribute__((ext_vector_type(8)));
typedef short s4 __attribute__((ext_vector_type(4)));
typedef __bf16 bf2 __attribute__((ext_vector_type(2)));
typedef __amdgpu_buffer_rsrc_t rsrc_t;
typedef unsigned long long u64;

constexpr int SEQ = 2048, DM = 640, FF = 1536;
constexpr float LN_EPS = 1e-5f;
constexpr float LOG2E = 1.4426950408889634f;

constexpr int OFF_EP = 0;
constexpr int OFF_ABORT = 256;
constexpr int OFF_FB = 4096;
constexpr int OFF_FLAG = 8192;
constexpr int FG_A1 = 0;
constexpr int FG_A2 = 32;
constexpr int FG_L = 64;
constexpr int FG_KV = 0, FG_O = 32, FG_X2 = 64, FG_H = 96, FG_X1 = 128, FG_H2 = 160, FG_X3 = 192, FG_PER_L = 224;
constexpr int OFF_WQ = 1 << 18;
constexpr int SZ_WQ = 32 * 4 * 80 * 256;
constexpr int OFF_WO = OFF_WQ + SZ_WQ;
constexpr int SZ_W6 = 40 * 80 * 256;
constexpr int OFF_W1 = OFF_WO + SZ_W6;
constexpr int OFF_W2 = OFF_W1 + SZ_W6;
constexpr int OFF_W3 = OFF_W2 + SZ_W6;
constexpr int SZ_W3 = 96 * 80 * 256;
constexpr int OFF_W4 = OFF_W3 + SZ_W3;
constexpr int SZ_W4 = 40 * 192 * 256;
constexpr int OFF_CSQ = OFF_W4 + SZ_W4;
constexpr int OFF_CS1 = OFF_CSQ + 32 * 8 * 64 * 4;
constexpr int OFF_CS3 = OFF_CS1 + 20 * 640 * 4;
constexpr int TILE6 = 80 * 256;
constexpr int TILEF = 192 * 256;
constexpr int SZ_A6 = 128 * TILE6, SZ_AF = 128 * TILEF;
constexpr int SZ_ST = 128 * 1024;
constexpr int SZ_KVP = 32 * 8 * 6144;
constexpr int OFF_KVP = OFF_CS3 + 20 * 1536 * 4;
constexpr int OFF_ACT = OFF_KVP + 2 * SZ_KVP;
constexpr int A_O = 0, A_X2 = A_O + SZ_A6, A_H = A_X2 + SZ_A6, A_X1 = A_H + SZ_A6, A_X3 = A_X1 + SZ_A6, A_H2 = A_X3 + SZ_A6,
              A_S2 = A_H2 + SZ_AF, A_S1 = A_S2 + SZ_ST, A_S3 = A_S1 + SZ_ST, SZ_LAYER = A_S3 + SZ_ST;
constexpr int LAYER_STRIDE = 0;
constexpr int OFF_PROF = OFF_ACT + SZ_LAYER + LAYER_STRIDE;
constexpr int WS_USED = OFF_PROF + 256 * 64 * 8;
constexpr unsigned OOB = 0x80000000u;

constexpr int L_BIG = 0;
constexpr int L_RED = 101376;
constexpr int L_SROW = 81920;
constexpr int SROW = 168;
constexpr int L_KVW = 0;
constexpr int L_KB = 32768;
constexpr int L_XROW = 81920;
constexpr int L_AROW = 98304;
constexpr int L_BROW = 112640;
constexpr int L_SMALL = 162816;
constexpr int L_CSQ = L_SMALL;
constexpr int L_CS1 = L_CSQ + 256;
constexpr int L_CS3 = L_CS1 + 320;
constexpr int LDS_BYTES = 163840;

constexpr int CODE_LINES = 380;
#define G5D 8
#define P1LK 5
#define P6LK 6
#define P6WD 1

constexpr int AUX_SC1 = 16;
constexpr unsigned SPIN_CAP = 1u << 17;

struct Params {
  const unsigned short* x;
  const unsigned short* wqkv;
  const unsigned short* wphi;
  const unsigned short* wo;
  const unsigned short* w1;
  const unsigned short* w2;
  const unsigned short* w3;
  const unsigned short* w4;
  unsigned short* out;
  char* ws;
};

DI f4 mfma(v4u a, v4u b, f4 c) {
  return __builtin_amdgcn_mfma_f32_16x16x32_bf16(__builtin_bit_cast(s8, a), __builtin_bit_cast(s8, b), c, 0, 0, 0);
}
DI unsigned pk2(float a, float b) {
  bf2 v = {(__bf16)a, (__bf16)b};
  return __builtin_bit_cast(unsigned, v);
}
DI float blo(unsigned u) { return __builtin_bit_cast(float, u << 16); }
DI float bhi(unsigned u) { return __builtin_bit_cast(float, u & 0xffff0000u); }
DI v4u ld16(rsrc_t r, unsigned voff, int soff) { return __builtin_amdgcn_raw_buffer_load_b128(r, voff, soff, AUX_SC1); }
DI v2u ld8(rsrc_t r, unsigned voff, int soff) { return __builtin_amdgcn_raw_buffer_load_b64(r, voff, soff, AUX_SC1); }
DI unsigned ld4(rsrc_t r, unsigned voff, int soff) { return __builtin_amdgcn_raw_buffer_load_b32(r, voff, soff, AUX_SC1); }
DI v4u ld16p(rsrc_t r, unsigned voff, int soff) { return __builtin_amdgcn_raw_buffer_load_b128(r, voff, soff, 0); }
DI v2u ld8p(rsrc_t r, unsigned voff, int soff) { return __builtin_amdgcn_raw_buffer_load_b64(r, voff, soff, 0); }
DI void st16(rsrc_t r, unsigned voff, int soff, v4u v) { __builtin_amdgcn_raw_buffer_store_b128(v, r, voff + (unsigned)soff, 0, AUX_SC1); }
DI void st8(rsrc_t r, unsigned voff, int soff, v2u v) { __builtin_amdgcn_raw_buffer_store_b64(v, r, voff, soff, AUX_SC1); }
DI void st4(rsrc_t r, unsigned voff, int soff, unsigned v) { __builtin_amdgcn_raw_buffer_store_b32(v, r, voff, soff, AUX_SC1); }
DI void dma16(rsrc_t r, LAS char* dst, unsigned voff, int soff) { __builtin_amdgcn_raw_ptr_buffer_load_lds(r, (LAS void*)dst, 16, voff, soff, 0, AUX_SC1); }
DI float asf(unsigned u) { return __builtin_bit_cast(float, u); }
DI unsigned asu(float f) { return __builtin_bit_cast(unsigned, f); }
DI f4 asf4(v4u u) { return __builtin_bit_cast(f4, u); }
DI v4u asu4(f4 f) { return __builtin_bit_cast(v4u, f); }

DI float xsum(float x) {
  auto a = __builtin_amdgcn_permlane16_swap(asu(x), asu(x), false, false);
  x = asf(a[0]) + asf(a[1]);
  auto b = __builtin_amdgcn_permlane32_swap(asu(x), asu(x), false, false);
  return asf(b[0]) + asf(b[1]);
}
DI void xsum2(float& X, float& Y) {
  auto a = __builtin_amdgcn_permlane32_swap(asu(X), asu(Y), false, false);
  float c = asf(a[0]) + asf(a[1]);
  auto d = __builtin_amdgcn_permlane16_swap(asu(c), asu(c), false, false);
  float e = asf(d[0]) + asf(d[1]);
  auto f = __builtin_amdgcn_permlane32_swap(asu(e), asu(e), false, false);
  X = asf(f[0]);
  Y = asf(f[1]);
}
DI void xmax2(float& X, float& Y, float inf) {
  auto a = __builtin_amdgcn_permlane32_swap(asu(X), asu(Y), false, false);
  float c = __builtin_amdgcn_fmed3f(asf(a[0]), asf(a[1]), inf);
  auto d = __builtin_amdgcn_permlane16_swap(asu(c), asu(c), false, false);
  float e = __builtin_amdgcn_fmed3f(asf(d[0]), asf(d[1]), inf);
  auto f = __builtin_amdgcn_permlane32_swap(asu(e), asu(e), false, false);
  X = asf(f[0]);
  Y = asf(f[1]);
}
struct GeluK { f2 c0, c1, one; };
DI GeluK gelu_consts() {
  GeluK k = {f2{-2.302208198f, -2.302208198f}, f2{-0.1029432397f, -0.1029432397f}, f2{1.0f, 1.0f}};
  asm volatile("" : "+v"(k.c0), "+v"(k.c1), "+v"(k.one));
  return k;
}
DI f2 gelu2(f2 x, const GeluK& k) {
  const f2 t = __builtin_elementwise_fma(x * x, k.c1, k.c0);
  const f2 a = x * t;
  const f2 d = f2{__builtin_amdgcn_exp2f(a[0]), __builtin_amdgcn_exp2f(a[1])} + k.one;
  return x * f2{__builtin_amdgcn_rcpf(d[0]), __builtin_amdgcn_rcpf(d[1])};
}
template <int N> DI void gelu_n(f2 (&x)[N], const GeluK& k) {
  f2 a[N];
#pragma unroll
  for (int i = 0; i < N; ++i) a[i] = x[i] * __builtin_elementwise_fma(x[i] * x[i], k.c1, k.c0);
#pragma unroll
  for (int i = 0; i < N; ++i) a[i] = f2{__builtin_amdgcn_exp2f(a[i][0]), __builtin_amdgcn_exp2f(a[i][1])};
#pragma unroll
  for (int i = 0; i < N; ++i) a[i] += k.one;
#pragma unroll
  for (int i = 0; i < N; ++i) a[i] = f2{__builtin_amdgcn_rcpf(a[i][0]), __builtin_amdgcn_rcpf(a[i][1])};
#pragma unroll
  for (int i = 0; i < N; ++i) x[i] *= a[i];
}
DI f2 lo2(f4 v) { return f2{v[0], v[1]}; }
DI f2 hi2(f4 v) { return f2{v[2], v[3]}; }

#define SB() __builtin_amdgcn_sched_barrier(0)
#define G5STB -1
#define GLSTB -1
#define FSTAMP(i) do {} while (0)
#define DRAIN() asm volatile("s_waitcnt vmcnt(0)" ::: "memory")
#define BAR() __syncthreads()
#define RAWBAR() do { SB(); asm volatile("s_waitcnt lgkmcnt(0)" ::: "memory"); __builtin_amdgcn_s_barrier(); asm volatile("" ::: "memory"); SB(); } while (0)

struct Ctx {
  rsrc_t ws;
  u64 tag;
  char* wsp;
  int lane, w, tok, g;
};

DI GAS u64* flag_ptr(const Ctx& c, int group, int idx) { return (GAS u64*)(c.wsp + OFF_FLAG + group * 256 + idx * 8); }
DI void publish(const Ctx& c, int group, int idx) {
  DRAIN();
  RAWBAR();
  if (threadIdx.x == 0) __hip_atomic_store(flag_ptr(c, group, idx), c.tag, __ATOMIC_RELAXED, __HIP_MEMORY_SCOPE_AGENT);
}
template <int NAFTER> DI void publish_n(const Ctx& c, int group, int idx) {
  asm volatile("s_waitcnt vmcnt(%0)" :: "n"(NAFTER) : "memory");
  RAWBAR();
  if (threadIdx.x == 0) __hip_atomic_store(flag_ptr(c, group, idx), c.tag, __ATOMIC_RELAXED, __HIP_MEMORY_SCOPE_AGENT);
}
DI void wait_flags(const Ctx& c, int byte_off, bool active) {
  GAS u64* p = (GAS u64*)(c.wsp + byte_off);
  GAS u64* ab = (GAS u64*)(c.wsp + OFF_ABORT);
  for (unsigned spins = 0;;) {
    u64 v = c.tag;
    if (active) v = __hip_atomic_load(p, __ATOMIC_RELAXED, __HIP_MEMORY_SCOPE_AGENT);
    if (__all(v == c.tag)) break;
    ++spins;
    if ((spins & 63u) == 0u) {
      u64 a = __hip_atomic_load(ab, __ATOMIC_RELAXED, __HIP_MEMORY_SCOPE_AGENT);
      if (__any(a == c.tag)) break;
      if (spins > SPIN_CAP) {
        __hip_atomic_store(ab, c.tag, __ATOMIC_RELAXED, __HIP_MEMORY_SCOPE_AGENT);
        break;
      }
    }
  }
  __builtin_amdgcn_fence(__ATOMIC_ACQUIRE, "wavefront");
  asm volatile("" ::: "memory");
}
DI void wait_group(const Ctx& c, int group, int first, int n) {
  wait_flags(c, OFF_FLAG + group * 256 + (first + (c.lane < n ? c.lane : 0)) * 8, c.lane < n);
}

#define STAMP(i) do {} while (0)
#define CSTAMP(i) do {} while (0)
#define SPAN(i) do {} while (0)

DI v4u gather8(const unsigned short* p, int stride) {
  unsigned short e[8];
#pragma unroll
  for (int j = 0; j < 8; ++j) e[j] = p[(size_t)j * stride];
  v4u v;
  v.x = e[0] | ((unsigned)e[1] << 16);
  v.y = e[2] | ((unsigned)e[3] << 16);
  v.z = e[4] | ((unsigned)e[5] << 16);
  v.w = e[6] | ((unsigned)e[7] << 16);
  return v;
}
DI float sum8(v4u v) { return ((blo(v.x) + bhi(v.x)) + (blo(v.y) + bhi(v.y))) + ((blo(v.z) + bhi(v.z)) + (blo(v.w) + bhi(v.w))); }

struct TaskB { const unsigned short* src; int stride; int cpr; int nfrag; unsigned dst; int kg; unsigned cs; };
DI TaskB decode_b(const Params& P, int t, bool on) {
  TaskB k;
  int kq, sl, base, csoff = -1;
  if (t < 480) {
    const int m = t / 160, tt = t - 160 * m;
    k.src = m == 0 ? P.wo : (m == 1 ? P.w1 : P.w2);
    k.stride = 640; kq = tt >> 3; sl = tt & 7; k.cpr = 10; k.nfrag = 5; k.kg = 80; base = OFF_WO + m * SZ_W6;
    if (m == 1) csoff = OFF_CS1 + (kq * 640 + 80 * sl) * 4;
  } else if (t < 800) {
    const int tt = t - 480;
    k.src = P.w3; k.stride = 1536; kq = tt >> 4; sl = tt & 15; k.cpr = 12; k.nfrag = 6; k.kg = 80; base = OFF_W3;
    csoff = OFF_CS3 + (kq * 1536 + 96 * sl) * 4;
  } else {
    const int tt = t - 800;
    k.src = P.w4; k.stride = 640; kq = tt >> 3; sl = tt & 7; k.cpr = 10; k.nfrag = 5; k.kg = 192; base = OFF_W4;
  }
  k.src += (size_t)(32 * kq) * k.stride + sl * (8 * k.cpr);
  k.dst = on ? (unsigned)(base + (sl * k.nfrag * k.kg + 4 * kq) * 256) : OOB;
  k.cs = (on && csoff >= 0) ? (unsigned)csoff : OOB;
  return k;
}
constexpr int RB_ROW = 208;
DI void task_load(const TaskB& k, int lane, v4u (&r)[6], int (&lo)[6]) {
  const int magic = k.cpr == 10 ? 6554 : 5462;
#pragma unroll
  for (int i = 0; i < 6; ++i) {
    const int ch = i * 64 + lane;
    const int row = (ch * magic) >> 16, cc = ch - row * k.cpr;
    const bool ok = row < 32;
    lo[i] = ok ? row * RB_ROW + cc * 16 : -1;
    r[i] = *(const GAS v4u*)((const GAS char*)k.src + (ok ? (unsigned)(row * (k.stride * 2) + cc * 16) : 0u));
  }
}
DI void task_store(const Ctx& c, const TaskB& k, LAS char* st, const v4u (&r)[6], const int (&lo)[6]) {
#pragma unroll
  for (int i = 0; i < 6; ++i)
    if (lo[i] >= 0) *(LAS v4u*)(st + lo[i]) = r[i];
  asm volatile("s_waitcnt lgkmcnt(0)" ::: "memory");
  const int q = c.tok >> 2, pc = c.tok & 3;
  const LAS char* r0 = st + (8 * c.g + q) * RB_ROW;
  v4u f[6];
#pragma unroll
  for (int nt = 0; nt < 6; ++nt) {
    const bool last = nt == 4 && k.cpr == 10;
    const int cb = last ? (64 + 4 * pc) * 2 : (32 * (nt >> 1) + 8 * pc + 4 * (nt & 1)) * 2;
    const LAS char* a = r0 + (nt < k.nfrag ? cb : 0);
    const s4 l4 = __builtin_amdgcn_ds_read_tr16_b64_v4i16((LAS s4*)a);
    const s4 h4 = __builtin_amdgcn_ds_read_tr16_b64_v4i16((LAS s4*)(a + 4 * RB_ROW));
    f[nt] = __builtin_bit_cast(v4u, __builtin_shufflevector(l4, h4, 0, 1, 2, 3, 4, 5, 6, 7));
  }
  const unsigned d0 = k.dst + (unsigned)(c.g * 256 + c.tok * 16);
#pragma unroll
  for (int nt = 0; nt < 6; ++nt) st16(c.ws, nt < k.nfrag ? d0 + (unsigned)(nt * k.kg * 256) : OOB, 0, f[nt]);
  if (k.cs != OOB) {
#pragma unroll
    for (int nt = 0; nt < 6; ++nt) {
      const float cs = xsum(sum8(f[nt]));
      st4(c.ws, (nt < k.nfrag && c.g == 0) ? k.cs + (unsigned)((nt * 16 + c.tok) * 4) : OOB, 0, asu(cs));
    }
  }
  asm volatile("s_waitcnt lgkmcnt(0)" ::: "memory");
}

struct G5 { unsigned voff[4]; int so[3]; unsigned red[3]; unsigned src; };
template <int KS> DI G5 gemm5_prep(const Ctx& c, int xoff, int tile, int p, int shift) {
  G5 a;
#pragma unroll
  for (int pos = 0; pos < 4; ++pos) {
    const int tt = (c.w + 1 + pos) & 3;
    const int t = 16 * (4 * p + tt) + c.tok + shift;
    a.voff[pos] = (t >= 0 && t < SEQ) ? (unsigned)((t >> 4) * tile + c.g * 256 + (t & 15) * 16) : OOB;
    if (pos < 3) a.red[pos] = L_RED + ((tt * 3 + (2 - pos)) * 5) * 1024 + c.lane * 16;
  }
  a.src = L_RED + (c.w * 15) * 1024 + c.lane * 16;
  int so = xoff + KS * c.w * 1024;
#pragma unroll
  for (int i = 0; i < 3; ++i) a.so[i] = __builtin_amdgcn_readfirstlane(so + i * 4096);
  asm volatile("" : "+v"(a.voff[0]), "+v"(a.voff[1]), "+v"(a.voff[2]), "+v"(a.voff[3]), "+v"(a.red[0]), "+v"(a.red[1]), "+v"(a.red[2]), "+v"(a.src));
  asm volatile("" : "+s"(a.so[0]), "+s"(a.so[1]), "+s"(a.so[2]));
  return a;
}
template <int KS, int D, int STB = -1, class F>
DI void gemm5(const Ctx& c0, LAS char* lds, const G5& a, const v4u (&wf)[5][KS], f4 (&out)[5], F slot) {
  const Ctx& c = c0;
  v4u xf[4 * KS];
#define G5X(k) xf[k] = ld16(c.ws, a.voff[(k) / KS] + (((k) % KS) & 3) * 1024, a.so[((k) % KS) >> 2])
#pragma unroll
  for (int k = 0; k < D; ++k) { G5X(k); SB(); }
  slot(-1);
  SB();
  if (STB >= 0) STAMP(STB);
  f4 acc[2][5];
#pragma unroll
  for (int pos = 0; pos < 4; ++pos) {
#pragma unroll
    for (int nt = 0; nt < 5; ++nt) acc[pos & 1][nt] = f4{0.f, 0.f, 0.f, 0.f};
#pragma unroll
    for (int u = 0; u < KS; ++u) {
      const int k = pos * KS + u;
#pragma unroll
      for (int nt = 0; nt < 5; ++nt) acc[pos & 1][nt] = mfma(wf[nt][u], xf[k], acc[pos & 1][nt]);
      if (k + D < 4 * KS) G5X(k + D);
      slot(k);
      if (pos >= 1) {
#pragma unroll
        for (int nt = 0; nt < 5; ++nt)
          if (nt * KS / 5 == u) *(LAS f4*)(lds + a.red[pos - 1] + nt * 1024) = acc[(pos - 1) & 1][nt];
      }
      __builtin_amdgcn_sched_group_barrier(0x008, 2, 0);
      __builtin_amdgcn_sched_group_barrier(0x020, 1, 0);
      __builtin_amdgcn_sched_group_barrier(0x008, 1, 0);
      __builtin_amdgcn_sched_group_barrier(0x080, 1, 0);
      __builtin_amdgcn_sched_group_barrier(0x008, 1, 0);
      __builtin_amdgcn_sched_group_barrier(0x020, 1, 0);
      __builtin_amdgcn_sched_group_barrier(0x008, 1, 0);
      SB();
    }
    if (STB >= 0 && pos == 0) STAMP(STB + 1);
    if (STB >= 0 && pos == 2) STAMP(STB + 2);
  }
#undef G5X
  if (STB >= 0) STAMP(STB + 3);
  RAWBAR();
  f4 part[15];
#pragma unroll
  for (int e = 0; e < 15; ++e) part[e] = *(const LAS f4*)(lds + a.src + e * 1024);
  SB();
  if (STB >= 0) STAMP(STB + 4);
#pragma unroll
  for (int nt = 0; nt < 5; ++nt) out[nt] = (acc[1][nt] + part[nt]) + (part[5 + nt] + part[10 + nt]);
}

template <int NT, int NTT, int LK, int STB = -1, class FX, class FE, class FI>
DI void gemm_lds(const Ctx& c0, const LAS char* wl, f4 (&acc)[NT][NTT], FX xload, FE extra, FI shadow) {
  constexpr int D = NT == 6 ? P6WD : 2;
  v4u xf[LK][NTT];
#pragma unroll
  for (int t = 0; t < LK; ++t)
#pragma unroll
    for (int t2 = 0; t2 < NTT; ++t2) { xf[t][t2] = xload(t, t2); SB(); }
  shadow();
  SB();
  if (STB >= 0) STAMP(STB);
  RAWBAR();
  if (STB >= 0) STAMP(STB + 1);
  v4u wb[D + 1][NT];
#pragma unroll
  for (int d = 0; d < D; ++d)
#pragma unroll
    for (int nt = 0; nt < NT; ++nt) wb[d][nt] = *(const LAS v4u*)(wl + (nt * 20 + d) * 1024);
#pragma unroll
  for (int nt = 0; nt < NT; ++nt)
#pragma unroll
    for (int t2 = 0; t2 < NTT; ++t2) acc[nt][t2] = f4{0.f, 0.f, 0.f, 0.f};
  SB();
#pragma unroll
  for (int t = 0; t < 20; ++t) {
    {
      v4u(&xs)[NTT] = xf[t % LK];
      v4u(&wsn)[NT] = wb[t % (D + 1)];
      if constexpr (NT == 4 && NTT == 4)
        asm volatile("" : "+v"(xs[0]), "+v"(xs[1]), "+v"(xs[2]), "+v"(xs[3]), "+v"(wsn[0]), "+v"(wsn[1]), "+v"(wsn[2]), "+v"(wsn[3]));
      else if constexpr (NT == 6 && NTT == 2)
        asm volatile("" : "+v"(xs[0]), "+v"(xs[1]), "+v"(wsn[0]), "+v"(wsn[1]), "+v"(wsn[2]), "+v"(wsn[3]), "+v"(wsn[4]), "+v"(wsn[5]));
    }
    SB();
#pragma unroll
    for (int nt = 0; nt < NT; ++nt) {
      if (t + D < 20) wb[(t + D) % (D + 1)][nt] = *(const LAS v4u*)(wl + (nt * 20 + t + D) * 1024);
#pragma unroll
      for (int t2 = 0; t2 < NTT; ++t2) acc[nt][t2] = mfma(wb[t % (D + 1)][nt], xf[t % LK][t2], acc[nt][t2]);
      if (t >= 1 && t - 1 + LK < 20 && nt < NTT) xf[(t - 1) % LK][nt] = xload(t - 1 + LK, nt);
      extra(t, nt);
      __builtin_amdgcn_sched_group_barrier(0x008, NTT / 2, 0);
      __builtin_amdgcn_sched_group_barrier(0x100, 1, 0);
      __builtin_amdgcn_sched_group_barrier(0x008, NTT - NTT / 2, 0);
      __builtin_amdgcn_sched_group_barrier(0x020, 1, 0);
    }
    SB();
    if (STB >= 0 && (t + 1) % 5 == 0) STAMP(STB + 1 + (t + 1) / 5);
  }
}

DI void pack5(const f4 (&v)[5], unsigned (&u)[10]) {
#pragma unroll
  for (int nt = 0; nt < 5; ++nt) {
    u[2 * nt] = pk2(v[nt][0], v[nt][1]);
    u[2 * nt + 1] = pk2(v[nt][2], v[nt][3]);
  }
}
DI void store5u(const Ctx& c, const unsigned (&u)[10], int aoff, int tt_g, int j) {
  const int so = aoff + tt_g * TILE6 + 10 * j * 256;
  const unsigned vo = c.g * 256 + c.tok * 16 + so;
  st16(c.ws, vo, 0, v4u{u[0], u[1], u[2], u[3]});
  st16(c.ws, vo, 1024, v4u{u[4], u[5], u[6], u[7]});
  st8(c.ws, (c.g >> 1) * 256 + c.tok * 16 + (c.g & 1) * 8, so + 2048, v2u{u[8], u[9]});
}
DI void row_stats(const f4 (&v)[5], float& s, float& q) {
  f2 s2 = lo2(v[0]) + hi2(v[0]), q2 = __builtin_elementwise_fma(hi2(v[0]), hi2(v[0]), lo2(v[0]) * lo2(v[0]));
#pragma unroll
  for (int nt = 1; nt < 5; ++nt) {
    s2 += lo2(v[nt]);
    s2 += hi2(v[nt]);
    q2 = __builtin_elementwise_fma(lo2(v[nt]), lo2(v[nt]), q2);
    q2 = __builtin_elementwise_fma(hi2(v[nt]), hi2(v[nt]), q2);
  }
  s = s2[0] + s2[1];
  q = q2[0] + q2[1];
  xsum2(s, q);
}
DI void store_sq(const Ctx& c, float s, float q, int soff, int tt_g, int j) {
  st8(c.ws, c.g == 0 ? (unsigned)(c.tok * 64 + j * 8) : OOB, soff + tt_g * 1024, v2u{asu(s), asu(q)});
}
DI void keep(v4u v) { asm volatile("" :: "v"(v)); }
template <int N, int NV> DI void spread_loads() {
#pragma unroll
  for (int i = 0; i < N; ++i) {
    __builtin_amdgcn_sched_group_barrier(0x402, NV, 0);
    __builtin_amdgcn_sched_group_barrier(0x020, 1, 0);
  }
  __builtin_amdgcn_sched_barrier(0);
}
DI void warm5(const Ctx& c, int aoff, int tt_g, int j, v4u (&wv)[3]) {
  const int so = aoff + tt_g * TILE6 + 10 * j * 256;
  wv[0] = ld16(c.ws, c.lane * 16, so);
  wv[1] = ld16(c.ws, c.lane * 16, so + 1024);
  wv[2] = ld16(c.ws, c.lane < 32 ? (unsigned)(c.lane * 16) : OOB, so + 2048);
}
DI void stats_from(v4u sv, float& mean, float& var) {
  float s = asf(sv.x) + asf(sv.z), q = asf(sv.y) + asf(sv.w);
  xsum2(s, q);
  mean = s * (1.0f / DM);
  var = __builtin_fmaxf(q * (1.0f / DM) - mean * mean, 0.f);
}

template <int NQ> DI void features(const f4 (&p0)[NQ], const f4 (&p1)[NQ], const float* lr, float inf, unsigned (&ph)[NQ][5]) {
  f2 ua[NQ], ub[NQ], uc[NQ], of[NQ];
#pragma unroll
  for (int q = 0; q < NQ; ++q) {
    const f2 l2 = f2{lr[q], lr[q]};
    ua[q] = lo2(p0[q]) * l2;
    ub[q] = hi2(p0[q]) * l2;
    uc[q] = f2{p1[q][0] * lr[q], 0.f};
    float mx = __builtin_fmaxf(__builtin_fmaxf(__builtin_fmaxf(ua[q][0], ua[q][1]), ub[q][0]), __builtin_fmaxf(ub[q][1], uc[q][0]));
    float mn = __builtin_fmaxf(__builtin_fmaxf(__builtin_fmaxf(-ua[q][0], -ua[q][1]), -ub[q][0]), __builtin_fmaxf(-ub[q][1], -uc[q][0]));
    of[q] = f2{mx, mn};
  }
#pragma unroll
  for (int q = 0; q < NQ; ++q) {
    float mx = of[q][0], mn = of[q][1];
    xmax2(mx, mn, inf);
    of[q] = f2{mx, mn};
  }
  f2 e[NQ][5];
  float sp[NQ], sn[NQ];
#pragma unroll
  for (int q = 0; q < NQ; ++q) {
    asm("v_pk_add_f32 %0, %5, %8 op_sel:[0,0] op_sel_hi:[0,1] neg_lo:[0,1] neg_hi:[1,1]\n\t"
        "v_pk_add_f32 %1, %5, %8 op_sel:[1,0] op_sel_hi:[1,1] neg_lo:[0,1] neg_hi:[1,1]\n\t"
        "v_pk_add_f32 %2, %6, %8 op_sel:[0,0] op_sel_hi:[0,1] neg_lo:[0,1] neg_hi:[1,1]\n\t"
        "v_pk_add_f32 %3, %6, %8 op_sel:[1,0] op_sel_hi:[1,1] neg_lo:[0,1] neg_hi:[1,1]\n\t"
        "v_pk_add_f32 %4, %7, %8 op_sel:[0,0] op_sel_hi:[0,1] neg_lo:[0,1] neg_hi:[1,1]"
        : "=&v"(e[q][0]), "=&v"(e[q][1]), "=&v"(e[q][2]), "=&v"(e[q][3]), "=&v"(e[q][4])
        : "v"(ua[q]), "v"(ub[q]), "v"(uc[q]), "v"(of[q]));
  }
#pragma unroll
  for (int q = 0; q < NQ; ++q) {
#pragma unroll
    for (int i = 0; i < 5; ++i) e[q][i] = f2{__builtin_amdgcn_exp2f(e[q][i][0]), __builtin_amdgcn_exp2f(e[q][i][1])};
    const f2 sm = ((e[q][0] + e[q][1]) + (e[q][2] + e[q][3])) + e[q][4];
    sp[q] = sm[0];
    sn[q] = sm[1];
  }
#pragma unroll
  for (int q = 0; q < NQ; ++q) xsum2(sp[q], sn[q]);
#pragma unroll
  for (int q = 0; q < NQ; ++q) {
    const f2 inv = f2{__builtin_amdgcn_rcpf(sp[q]), __builtin_amdgcn_rcpf(sn[q])};
#pragma unroll
    for (int i = 0; i < 5; ++i) {
      const f2 y = e[q][i] * inv;
      ph[q][i] = pk2(y[0], y[1]);
    }
  }
}

#define HK_F 2
#define MAP_P(x, i) (8 * ((x) >> 1) + ((i) >> 2))
#define MAP_J(x, i) (4 * ((x) & 1) + ((i) & 3))
#define MAP_G(x, i) (2 * ((x) >> 1) + ((i) >> 4))
#define MAP_H(x, i) (16 * ((x) & 1) + ((i) & 15))
#define MAP_Q(x, i) (4 * ((x) >> 1) + ((i) >> 3))
#define MAP_I(x, i) (8 * ((x) & 1) + ((i) & 7))
#define PHASE_IDS()                                                                     \
  Ctx c = c0;                                                                           \
  asm volatile("" : "+v"(c.lane));                                                      \
  asm volatile("" : "+s"(c.w), "+s"(c.tag));                                            \
  c.tok = c.lane & 15;                                                                  \
  c.g = c.lane >> 4;                                                                    \
  int bb_ = blockIdx.x;                                                                 \
  asm volatile("" : "+s"(bb_));                                                         \
  const int xg = bb_ & 7, idx = bb_ >> 3;                                               \
  const int p = MAP_P(xg, idx), j = MAP_J(xg, idx);                                     \
  const int G = MAP_G(xg, idx), h = MAP_H(xg, idx);                                     \
  const int q6 = MAP_Q(xg, idx), i6 = MAP_I(xg, idx);                                   \
  const int w = c.w, lane = c.lane, tok = c.tok, g = c.g;                               \
  const int tid = w * 64 + lane;                                                        \
  const int ttg = 4 * p + w;                                                            \
  (void)p; (void)j; (void)G; (void)h; (void)q6; (void)i6; (void)tok; (void)g; (void)tid; (void)ttg; (void)lane;

#define WOFF(x) (x)
#define W5(arr, off, i) arr[(i) % 5][(i) / 5] = ld16(c.ws, lane * 16 + (((i) / 5) & 3) * 1024, WOFF((off) + (5 * j + (i) % 5) * 20480 + 5120 * w + (((i) / 5) >> 2) * 4096))
#define W4(e) w4f[(e) % 5][(e) / 5] = ld16(c.ws, lane * 16 + (((e) / 5) & 3) * 1024, WOFF(OFF_W4 + (5 * j + (e) % 5) * 49152 + 12288 * w + (((e) / 5) >> 2) * 4096))
#define DMA_BASES()                                                                                   \
  int w3g = WOFF(OFF_W3 + (i6 * 120 + w) * 1024), wqg = WOFF(OFF_WQ + (h * 80 + w * 20) * 1024);      \
  int w3l = L_BIG + w * 1024, wql = L_BIG + w * 20480;                                                \
  asm volatile("" : "+s"(w3g), "+s"(wqg), "+s"(w3l), "+s"(wql));     \
  (void)w3g; (void)wqg; (void)w3l; (void)wql;
#define W3DMA(i) dma16(c.ws, lds + w3l + (i) * 4096, lane * 16, w3g + (i) * 4096)
#define WQDMA(u) dma16(c.ws, lds + wql + (u) * 1024, lane * 16, wqg + (u) * 1024)

__global__ void __launch_bounds__(256, 1) fused_kernel(Params P) {
  extern __shared__ __attribute__((aligned(16))) char smem[];
  LAS char* lds = (LAS char*)smem;
  Ctx c0;
  c0.wsp = P.ws;
  c0.ws = __builtin_amdgcn_make_buffer_rsrc((void*)P.ws, 0, WS_USED, 0x00020000);
  c0.lane = threadIdx.x & 63;
  c0.w = __builtin_amdgcn_readfirstlane(threadIdx.x >> 6);
  c0.tok = c0.lane & 15;
  c0.g = c0.lane >> 4;
  c0.tag = 0;
  STAMP(0);
  CSTAMP(62);
  SPAN(0);
  unsigned codepf[2];
  {
    u64 pc;
    asm volatile("s_getpc_b64 %0" : "=s"(pc));
    const GAS unsigned* cp = (const GAS unsigned*)(pc & ~(u64)127);
#pragma unroll
    for (int i = 0; i < 2; ++i) {
      const int line = (i * 4 + c0.w) * 64 + c0.lane;
      codepf[i] = cp[(line < CODE_LINES ? line : 0) * 32];
    }
  }
  v4u wph[2];
  f4 x2[5], x1[5];
  {
    PHASE_IDS();
    DMA_BASES();
    const rsrc_t rx = __builtin_amdgcn_make_buffer_rsrc((void*)P.x, 0, SEQ * DM * 2, 0x00020000);
    v2u ar[5];
    int alo[5];
#pragma unroll
    for (int i = 0; i < 5; ++i) {
      const int e = tid + 256 * i, row = (e * 4370) >> 16, pc = e - 15 * row, sec = (pc * 13) >> 6;
      const bool ok = e < 1200;
      alo[i] = ok ? row * 136 + pc * 8 : -1;
      ar[i] = *(const GAS v2u*)((const GAS char*)P.wqkv + 80 * G * 3840 + 40 * h + (ok ? (unsigned)(row * 3840 + sec * 1280 + (pc - 5 * sec) * 8) : 0u));
    }
    const int tk = 256 * G + 8 * h + 2 * w + (lane >> 5);
    v4u xr[3];
#pragma unroll
    for (int q = 0; q < 3; ++q) {
      const int ch = (lane & 31) + 32 * q;
      xr[q] = ld16p(rx, ch < 80 ? (unsigned)(tk * (DM * 2) + ch * 16) : OOB, 0);
    }
    const u64 ep = __hip_atomic_load((GAS u64*)(P.ws + OFF_EP), __ATOMIC_RELAXED, __HIP_MEMORY_SCOPE_AGENT);
    SB();
    {
#pragma unroll
      for (int i = 0; i < 5; ++i)
        if (alo[i] >= 0) *(LAS v2u*)(lds + L_AROW + alo[i]) = ar[i];
      float sm = 0.f, sq = 0.f;
#pragma unroll
      for (int q = 0; q < 3; ++q) {
        const int ch = (lane & 31) + 32 * q;
        if (ch < 80) *(LAS v4u*)(lds + L_XROW + (2 * w + (lane >> 5)) * 1296 + ch * 16) = xr[q];
        const unsigned e[4] = {xr[q].x, xr[q].y, xr[q].z, xr[q].w};
#pragma unroll
        for (int k = 0; k < 4; ++k) {
          const float a = blo(e[k]), b = bhi(e[k]);
          sm += a + b;
          sq += a * a + b * b;
        }
      }
      c0.tag = ((u64)__builtin_amdgcn_readfirstlane((unsigned)((ep + 1) >> 32)) << 32) | __builtin_amdgcn_readfirstlane((unsigned)(ep + 1));
      c.tag = c0.tag;
      RAWBAR();
      float cs = 0.f;
      {
        const int gp = tok >> 2, v = 4 * w + (tok & 3), sec = (v * 13) >> 6;
        const bool valid = v < 15;
        const int col = valid ? 20 * sec + 5 * gp + (v - 5 * sec) : 0;
        unsigned short e[3][8];
#pragma unroll
        for (int q = 0; q < 3; ++q) {
          const int kl = 4 * q + ((q == 2) ? (g & 1) : g);
          const LAS unsigned short* src = (const LAS unsigned short*)(lds + L_AROW + 8 * kl * 136) + col;
#pragma unroll
          for (int jj = 0; jj < 8; ++jj) e[q][jj] = src[jj * 68];
        }
        v4u xv[3];
#pragma unroll
        for (int q = 0; q < 3; ++q) {
          const int e2 = tid + 256 * q, grp = e2 >> 3, tk8 = e2 & 7;
          xv[q] = *(const LAS v4u*)(lds + L_XROW + tk8 * 1296 + (e2 < 640 ? grp : 0) * 16);
        }
        SB();
#pragma unroll
        for (int q = 0; q < 3; ++q) {
          const int e2 = tid + 256 * q, grp = e2 >> 3, tk8 = e2 & 7;
          st16(c.ws, e2 < 640 ? (unsigned)((16 * G + (h >> 1)) * TILE6 + grp * 256 + (8 * (h & 1) + tk8) * 16) : OOB, OFF_ACT + A_X3, xv[q]);
        }
#pragma unroll
        for (int q = 0; q < 3; ++q) {
          v4u f = v4u{e[q][0] | ((unsigned)e[q][1] << 16), e[q][2] | ((unsigned)e[q][3] << 16), e[q][4] | ((unsigned)e[q][5] << 16), e[q][6] | ((unsigned)e[q][7] << 16)};
          if (!valid) f = v4u{0u, 0u, 0u, 0u};
          const bool on = (q < 2) || (g < 2);
          st16(c.ws, on ? (unsigned)(((h * 4 + w) * 80 + 10 * G + 4 * q + g) * 256 + tok * 16) : OOB, OFF_WQ, f);
          cs += on ? sum8(f) : 0.f;
        }
      }
      cs = xsum(cs);
      st4(c.ws, g == 0 ? (unsigned)(((h * 8 + G) * 64 + w * 16 + tok) * 4) : OOB, OFF_CSQ, asu(cs));
#pragma unroll
      for (int o = 1; o < 32; o <<= 1) {
        sm += __shfl_xor(sm, o);
        sq += __shfl_xor(sq, o);
      }
      st16(c.ws, (lane & 31) < 4 ? (unsigned)(tk * 64 + (lane & 31) * 16) : OOB, OFF_ACT + A_S3,
           (lane & 31) == 0 ? v4u{asu(sm), asu(sq), 0u, 0u} : v4u{0u, 0u, 0u, 0u});
        DRAIN();
      RAWBAR();
      if (tid == 0) {
        __hip_atomic_store(flag_ptr(c, FG_A1 + h, G), c.tag, __ATOMIC_RELAXED, __HIP_MEMORY_SCOPE_AGENT);
        __hip_atomic_store(flag_ptr(c, FG_A2 + G * 4 + (h >> 3), h & 7), c.tag, __ATOMIC_RELAXED, __HIP_MEMORY_SCOPE_AGENT);
      }
    }
    STAMP(1);
    asm volatile("" :: "v"(codepf[0]), "v"(codepf[1]));
    const int wi = (xg * 32 + idx) * 4 + w;
    const bool two = wi % 6 == 0 && wi / 6 < 160;
    const TaskB k0 = decode_b(P, wi, true), k1 = decode_b(P, two ? 1024 + wi / 6 : wi, two);
    v4u rb0[6], rb1[6];
    int lo0[6], lo1[6];
    task_load(k0, lane, rb0, lo0);
    if (two) task_load(k1, lane, rb1, lo1);
    unsigned short we[2][5];
    {
      const int gp = tok >> 2, i = tok & 3;
#pragma unroll
      for (int R = 0; R < 2; ++R) {
        const int f = R == 0 ? 5 * gp + i : 5 * gp + 4;
#pragma unroll
        for (int jj = 0; jj < 5; ++jj) we[R][jj] = P.wphi[(h * 20 + 5 * g + jj) * 20 + f];
      }
    }
    const unsigned xvo = (unsigned)((16 * ttg + tok) * (DM * 2) + 160 * j);
    const v4u xa = ld16p(rx, xvo + g * 16, 0), xb = ld16p(rx, xvo + 64 + g * 16, 0);
    const v2u xe = ld8p(rx, xvo + 128 + g * 8, 0);
    SB();
    wait_flags(c, OFF_FLAG + (lane < 8 ? (FG_A1 + h) * 256 + lane * 8 : (FG_A2 + G * 4 + (((lane - 8) & 31) >> 3)) * 256 + ((lane - 8) & 7) * 8), lane < 40);
#pragma unroll
    for (int u = 0; u < 20; ++u) WQDMA(u);
    unsigned cp[8];
#pragma unroll
    for (int gg = 0; gg < 8; ++gg) cp[gg] = ld4(c.ws, lane * 4, OFF_CSQ + (h * 8 + gg) * 256);
    SB();
    task_store(c, k0, lds + L_BROW + w * 6656, rb0, lo0);
    if (two) task_store(c, k1, lds + L_BROW + w * 6656, rb1, lo1);
    {
      float cs = 0.f;
#pragma unroll
      for (int gg = 0; gg < 8; ++gg) cs += asf(cp[gg]);
      if (w == 0) *(LAS float*)(lds + L_CSQ + lane * 4) = cs;
      const int i = tok & 3;
#pragma unroll
      for (int R = 0; R < 2; ++R) {
        const bool rv = (R == 0) || (i == 0);
        wph[R] = rv ? v4u{we[R][0] | ((unsigned)we[R][1] << 16), we[R][2] | ((unsigned)we[R][3] << 16), (unsigned)we[R][4], 0u} : v4u{0u, 0u, 0u, 0u};
      }
      x2[0] = f4{blo(xa.x), bhi(xa.x), blo(xa.y), bhi(xa.y)};
      x2[1] = f4{blo(xa.z), bhi(xa.z), blo(xa.w), bhi(xa.w)};
      x2[2] = f4{blo(xb.x), bhi(xb.x), blo(xb.y), bhi(xb.y)};
      x2[3] = f4{blo(xb.z), bhi(xb.z), blo(xb.w), bhi(xb.w)};
      x2[4] = f4{blo(xe.x), bhi(xe.x), blo(xe.y), bhi(xe.y)};
    }
    DRAIN();
    RAWBAR();
    if (tid == 0) __hip_atomic_store((GAS u64*)(P.ws + OFF_FB) + blockIdx.x, c.tag, __ATOMIC_RELAXED, __HIP_MEMORY_SCOPE_AGENT);
    STAMP(2);
  }

  v4u wa[5][5];
#pragma unroll
  for (int i = 0; i < 12; ++i) wa[i % 5][i / 5] = v4u{0u, 0u, 0u, 0u};

#pragma unroll 1
  for (int L = 0; L < 2; ++L) {
    const int act = OFF_ACT + L * LAYER_STRIDE;
    const int fgl = FG_L + L * FG_PER_L;
    v4u wb[5][5], wv[6];
    v4u phq0[4];
    unsigned phq1[4];
    {
      PHASE_IDS();
      DMA_BASES();
      const int tt0 = 16 * G + 4 * w;
      const unsigned xv = lane * 16 + tt0 * TILE6;
      if (L == 1) wait_group(c, FG_L + FG_X3 + 4 * G + w, 0, 8);
      else DRAIN();
      STAMP(3 + 20 * L);
      v4u stv[4];
#pragma unroll
      for (int tt = 0; tt < 4; ++tt) stv[tt] = ld16(c.ws, tok * 64 + g * 16, OFF_ACT + A_S3 + (tt0 + tt) * 1024);
      SB();
      float mean[4], lr[4], rstd[4];
      f4 acc4[4][4];
      gemm_lds<4, 4, P1LK, GLSTB>(c, lds + L_BIG + lane * 16, acc4,
                           [&](int t, int t2) { return ld16(c.ws, xv + t2 * TILE6 + (t & 3) * 1024, OFF_ACT + A_X3 + (t >> 2) * 4096); },
                           [&](int, int) {},
                           [&]() {
#pragma unroll
                             for (int tt = 0; tt < 4; ++tt) {
                               float var;
                               stats_from(stv[tt], mean[tt], var);
                               rstd[tt] = __builtin_amdgcn_rsqf(var + LN_EPS);
                               asm volatile("" : "+v"(mean[tt]), "+v"(rstd[tt]));
                             }
                           });
      STAMP(4 + 20 * L);
      if (L == 0) {
        GAS u64* ab = (GAS u64*)(c.wsp + OFF_ABORT);
        for (unsigned spins = 0;;) {
          const v4u a = ld16(c.ws, lane * 32, OFF_FB), bq = ld16(c.ws, lane * 32 + 16, OFF_FB);
          const unsigned lo = (unsigned)c.tag, hi = (unsigned)(c.tag >> 32);
          const bool ok = a.x == lo && a.y == hi && a.z == lo && a.w == hi && bq.x == lo && bq.y == hi && bq.z == lo && bq.w == hi;
          if (__all(ok)) break;
          ++spins;
          if ((spins & 63u) == 0u) {
            u64 av = __hip_atomic_load(ab, __ATOMIC_RELAXED, __HIP_MEMORY_SCOPE_AGENT);
            if (__any(av == c.tag)) break;
            if (spins > SPIN_CAP) { __hip_atomic_store(ab, c.tag, __ATOMIC_RELAXED, __HIP_MEMORY_SCOPE_AGENT); break; }
          }
        }
        __builtin_amdgcn_fence(__ATOMIC_ACQUIRE, "wavefront");
        asm volatile("" ::: "memory");
#pragma unroll
        for (int i = 0; i < 12; ++i) W5(wa, OFF_WO, i);
      }
#pragma unroll
      for (int tt = 0; tt < 4; ++tt) lr[tt] = rstd[tt] * LOG2E;
      f4 csq[4];
#pragma unroll
      for (int nt = 0; nt < 4; ++nt) csq[nt] = *(const LAS f4*)(lds + L_CSQ + (nt * 16 + 4 * g) * 4);
      float inf = __builtin_inff();
      asm volatile("" : "+v"(inf));
      const f4 z4 = f4{0.f, 0.f, 0.f, 0.f};
      {
        f4 pk0[4], pk1[4];
        unsigned vb[4][3];
#pragma unroll
        for (int tt = 0; tt < 4; ++tt) {
          const f2 m2 = f2{-mean[tt], -mean[tt]}, r2 = f2{rstd[tt], rstd[tt]};

          const f2 v45 = __builtin_elementwise_fma(m2, lo2(csq[1]), lo2(acc4[1][tt]));
          const f2 v67 = __builtin_elementwise_fma(m2, hi2(csq[1]), hi2(acc4[1][tt]));
          const f2 v89 = __builtin_elementwise_fma(m2, lo2(csq[2]), lo2(acc4[2][tt]));
          const f2 vab = __builtin_elementwise_fma(m2, hi2(csq[2]), hi2(acc4[2][tt])) * r2;
          const f2 vcd = __builtin_elementwise_fma(m2, lo2(csq[3]), lo2(acc4[3][tt])) * r2;
          const float ve = __builtin_fmaf(-mean[tt], csq[3][2], acc4[3][tt][2]) * rstd[tt];
          const v4u bq = v4u{pk2(v45[1], v67[0]), pk2(v67[1], v89[0]), pk2(v89[1], 0.f), 0u};
          pk0[tt] = mfma(wph[0], bq, z4);
          pk1[tt] = mfma(wph[1], bq, z4);
          vb[tt][0] = pk2(vab[0], vab[1]);
          vb[tt][1] = pk2(vcd[0], vcd[1]);
          vb[tt][2] = pk2(ve, 1.0f);
        }
        unsigned ph[4][5];
        features<4>(pk0, pk1, lr, inf, ph);
#pragma unroll
        for (int tt = 0; tt < 4; ++tt) {
          LAS char* row = lds + L_SROW + (64 * w + 16 * tt + tok) * SROW;
          *(LAS v2u*)(row + g * 24) = v2u{ph[tt][0], ph[tt][1]};
          *(LAS v2u*)(row + g * 24 + 8) = v2u{ph[tt][2], ph[tt][3]};
          *(LAS v2u*)(row + g * 24 + 16) = v2u{ph[tt][4], 0u};
          *(LAS v2u*)(row + 96 + g * 16) = v2u{vb[tt][0], vb[tt][1]};
          *(LAS v2u*)(row + 96 + g * 16 + 8) = v2u{vb[tt][2], 0u};
        }
      }
      FSTAMP(49);
      RAWBAR();
      {
        f4 kv[3][2];
        const int qq = tok >> 2, pp = tok & 3;
        v4u fa[2][3], fb[2][2];
#pragma unroll
        for (int ks = 0; ks < 2; ++ks) {
          const LAS char* r0 = lds + L_SROW + (64 * w + 32 * ks + 8 * g + qq) * SROW + pp * 8;
#pragma unroll
          for (int a = 0; a < 3; ++a) {
            const s4 lo = __builtin_amdgcn_ds_read_tr16_b64_v4i16((LAS s4*)(r0 + a * 32));
            const s4 hi = __builtin_amdgcn_ds_read_tr16_b64_v4i16((LAS s4*)(r0 + 4 * SROW + a * 32));
            fa[ks][a] = __builtin_bit_cast(v4u, __builtin_shufflevector(lo, hi, 0, 1, 2, 3, 4, 5, 6, 7));
          }
#pragma unroll
          for (int bt = 0; bt < 2; ++bt) {
            const s4 lo = __builtin_amdgcn_ds_read_tr16_b64_v4i16((LAS s4*)(r0 + 96 + bt * 32));
            const s4 hi = __builtin_amdgcn_ds_read_tr16_b64_v4i16((LAS s4*)(r0 + 4 * SROW + 96 + bt * 32));
            fb[ks][bt] = __builtin_bit_cast(v4u, __builtin_shufflevector(lo, hi, 0, 1, 2, 3, 4, 5, 6, 7));
          }
        }
        SB();
#pragma unroll
        for (int a = 0; a < 3; ++a)
#pragma unroll
          for (int bt = 0; bt < 2; ++bt) {
            kv[a][bt] = mfma(fa[0][a], fb[0][bt], z4);
            kv[a][bt] = mfma(fa[1][a], fb[1][bt], kv[a][bt]);
          }
#pragma unroll
        for (int a = 0; a < 3; ++a)
#pragma unroll
          for (int bt = 0; bt < 2; ++bt) *(LAS f4*)(lds + L_KVW + (w * 6 + a * 2 + bt) * 1024 + lane * 16) = kv[a][bt];
      }
      RAWBAR();
      {
        const int e1 = 256 + (tid & 127);
        f4 t0[4], t1[4];
#pragma unroll
        for (int ww = 0; ww < 4; ++ww) {
          t0[ww] = *(const LAS f4*)(lds + L_KVW + ww * 6144 + tid * 16);
          t1[ww] = *(const LAS f4*)(lds + L_KVW + ww * 6144 + e1 * 16);
        }
        SB();
        const int ko = OFF_KVP + L * SZ_KVP + (h * 8 + G) * 6144;
        st16(c.ws, tid * 16, ko, asu4((t0[0] + t0[1]) + (t0[2] + t0[3])));
        st16(c.ws, tid < 128 ? (unsigned)(e1 * 16) : OOB, ko, asu4((t1[0] + t1[1]) + (t1[2] + t1[3])));
      }
      SB();
#pragma unroll
      for (int i = 0; i < 12; ++i) W5(wb, OFF_W1, i);
      SB();
      publish_n<12>(c, fgl + FG_KV + h, G);
      STAMP(5 + 20 * L);
      if (L == 0) {
        const int t = tid & 127;
        const bool three = w >= 2;
        const bool on = t < (three ? 96 : 80);
        const unsigned vo = on ? (unsigned)(((three ? 96 * i6 : 80 * j) + t) * 4) : OOB;
        const int so = three ? OFF_CS3 : OFF_CS1, st = three ? 1536 * 4 : 640 * 4;
        unsigned cp[20];
#pragma unroll
        for (int kq = 0; kq < 20; ++kq) cp[kq] = ld4(c.ws, vo, so + kq * st);
        SB();
        float cs = 0.f;
#pragma unroll
        for (int kq = 0; kq < 20; ++kq) cs += asf(cp[kq]);
        if (on) *(LAS float*)(lds + (three ? L_CS3 : L_CS1) + t * 4) = cs;
      }
      SB();
#pragma unroll
      for (int i = 12; i < 25; ++i) { W5(wa, OFF_WO, i); W5(wb, OFF_W1, i); }
      {
        f4 pq0[4], pq1[4];
#pragma unroll
        for (int tt = 0; tt < 4; ++tt) {
          const f2 m2 = f2{-mean[tt], -mean[tt]};

          const f2 v01 = __builtin_elementwise_fma(m2, lo2(csq[0]), lo2(acc4[0][tt]));
          const f2 v23 = __builtin_elementwise_fma(m2, hi2(csq[0]), hi2(acc4[0][tt]));
          const float v4 = __builtin_fmaf(-mean[tt], csq[1][0], acc4[1][tt][0]);
          const v4u bq = v4u{pk2(v01[0], v01[1]), pk2(v23[0], v23[1]), pk2(v4, 0.f), 0u};
          pq0[tt] = mfma(wph[0], bq, z4);
          pq1[tt] = mfma(wph[1], bq, z4);
        }
        unsigned ph[4][5];
        features<4>(pq0, pq1, lr, inf, ph);
#pragma unroll
        for (int tt = 0; tt < 4; ++tt) {
          phq0[tt] = v4u{ph[tt][0], ph[tt][1], ph[tt][2], ph[tt][3]};
          phq1[tt] = ph[tt][4];
        }
#pragma unroll
        for (int i = 0; i < 26; ++i) {
          __builtin_amdgcn_sched_group_barrier(0x402, 9, 0);
          __builtin_amdgcn_sched_group_barrier(0x020, 1, 0);
        }
        SB();
#pragma unroll
        for (int tt = 0; tt < 4; ++tt) asm volatile("" : "+v"(phq0[tt]), "+v"(phq1[tt]));
        FSTAMP(50);
      }
    }
    {
      PHASE_IDS();
      DMA_BASES();
      const bool odd = h & 1;
      const int e1 = 256 + (tid & 127);
      unsigned kbw[2], kbr[3];
#pragma unroll
      for (int rep = 0; rep < 2; ++rep) {
        const int e = rep ? e1 : tid, tl = e >> 6, ln = e & 63;
        const int pr = 8 * (tl >> 1) + 2 * (ln >> 4);
        kbw[rep] = L_KB + (16 * (tl & 1) + (ln & 15)) * 144 + (pr / 6) * 32 + (pr % 6) * 4;
      }
      {
        const int gp = tok >> 2, i = tok & 3;
#pragma unroll
        for (int R = 0; R < 3; ++R) {
          int bs = 5;
          if (R < 2) {
            const int d = odd ? (gp == 0 ? i : 8 * gp - 4 + 4 * R + i) : 8 * gp + 4 * R + i;
            const bool rv = odd ? (gp == 0 ? R == 0 : gp < 3) : d < 20;
            bs = rv ? 8 * (d / 5) + d % 5 : 6;
          }
          kbr[R] = L_KB + bs * 144 + g * 32;
        }
      }
      const bool full = odd ? (g == 1 || g == 2) : (g < 2);
      const bool half = odd ? (g == 0) : (g == 2);
      const int so0 = act + A_O + (16 * G + 4 * w) * TILE6 + ((20 * h) >> 3) * 256;
      const unsigned vfull = full ? (unsigned)(g * 256 + tok * 16 + so0) : OOB, vhalf = half ? (unsigned)(g * 256 + tok * 16 + (odd ? 8 : 0) + so0) : OOB;
      const unsigned v1 = tid < 128 ? (unsigned)(e1 * 16) : OOB;
      asm volatile("" :: "v"(kbw[0]), "v"(kbw[1]), "v"(kbr[0]), "v"(kbr[1]), "v"(kbr[2]), "v"(vfull), "v"(vhalf), "v"(v1));
      wait_group(c, fgl + FG_KV + h, 0, 8);
      STAMP(6 + 20 * L);
      {
        const int ko = OFF_KVP + L * SZ_KVP + h * 8 * 6144;
        v4u t0[8], t1[8];
#pragma unroll
        for (int gg = 0; gg < 8; ++gg) {
          t0[gg] = ld16(c.ws, tid * 16, ko + gg * 6144);
          t1[gg] = ld16(c.ws, v1, ko + gg * 6144);
        }
        SB();
#pragma unroll
        for (int rep = 0; rep < 2; ++rep) {
          const v4u* t = rep ? t1 : t0;
          const f4 s = ((asf4(t[0]) + asf4(t[1])) + (asf4(t[2]) + asf4(t[3]))) + ((asf4(t[4]) + asf4(t[5])) + (asf4(t[6]) + asf4(t[7])));
          if (rep == 0 || tid < 128) *(LAS v2u*)(lds + kbw[rep]) = v2u{pk2(s[0], s[1]), pk2(s[2], s[3])};
        }
      }
      FSTAMP(46);
      RAWBAR();
      v4u ka0[3];
      unsigned ka1[3];
#pragma unroll
      for (int R = 0; R < 3; ++R) {
        ka0[R] = *(const LAS v4u*)(lds + kbr[R]);
        ka1[R] = *(const LAS unsigned*)(lds + kbr[R] + 16);
      }
      f4 o[4][3];
#pragma unroll
      for (int tt = 0; tt < 4; ++tt)
#pragma unroll
        for (int R = 0; R < 3; ++R) o[tt][R] = mfma(ka0[R], phq0[tt], f4{0.f, 0.f, 0.f, 0.f});
#pragma unroll
      for (int tt = 0; tt < 4; ++tt)
#pragma unroll
        for (int R = 0; R < 3; ++R) o[tt][R] = mfma(v4u{ka1[R], 0u, 0u, 0u}, v4u{phq1[tt], 0u, 0u, 0u}, o[tt][R]);
#pragma unroll
      for (int tt = 0; tt < 4; ++tt) {
        const float inv = __builtin_amdgcn_rcpf(o[tt][2][0] + 1e-6f);
        const f2 i2 = f2{inv, inv};
        const f2 a = lo2(o[tt][0]) * i2, b = hi2(o[tt][0]) * i2, d = lo2(o[tt][1]) * i2, e = hi2(o[tt][1]) * i2;
        const unsigned u0 = pk2(a[0], a[1]), u1 = pk2(b[0], b[1]), u2 = pk2(d[0], d[1]), u3 = pk2(e[0], e[1]);
        st16(c.ws, vfull, tt * TILE6, v4u{u0, u1, u2, u3});
        st8(c.ws, vhalf, tt * TILE6, v2u{u0, u1});
      }
      FSTAMP(47);
      publish(c, fgl + FG_O + G * 4 + (h >> 3), h & 7);
#pragma unroll
      for (int e = 0; e < 2; ++e) wv[e] = ld16(c.ws, tok * 16 + g * TILE6 + (e + (odd ? 1 : 0)) * 256, so0);
      wv[2] = wv[0];
      STAMP(7 + 20 * L);
#pragma unroll
      for (int i = 0; i < 12; ++i) W3DMA(i);
    }
    {
      PHASE_IDS();
      DMA_BASES();
      const G5 ga = gemm5_prep<5>(c, act + A_O, TILE6, p, 0);
      wait_group(c, fgl + FG_O + (p >> 2) * 4 + w, 0, 8);
      keep(wv[0]); keep(wv[1]); keep(wv[2]);
      STAMP(8 + 20 * L);
      f4 o[5];
      gemm5<5, G5D, G5STB>(c, lds, ga, wa, o, [&](int k) {
        if (k >= 8) W3DMA(k + 4);
      });
#pragma unroll
      for (int i = 0; i < 12; ++i) W5(wa, OFF_W2, i);
#pragma unroll
      for (int nt = 0; nt < 5; ++nt) x2[nt] += o[nt];
      unsigned u[10];
      float rs, rq;
      pack5(x2, u);
      row_stats(x2, rs, rq);
      spread_loads<12, 6>();
      store5u(c, u, act + A_X2, ttg, j);
      store_sq(c, rs, rq, act + A_S2, ttg, j);
      publish_n<0>(c, fgl + FG_X2 + p, j);
      { v4u t_[3]; warm5(c, act + A_X2, ttg, j, t_); wv[0] = t_[0]; wv[1] = t_[1]; wv[2] = t_[2]; }
      STAMP(9 + 20 * L);
    }
    v4u w4f[5][12];
    float m2, r2;
    {
      PHASE_IDS();
      DMA_BASES();
      const GeluK gk = gelu_consts();
      const G5 ga = gemm5_prep<5>(c, act + A_X2, TILE6, p, 0);
      wait_group(c, fgl + FG_X2 + p, 0, 8);
      keep(wv[0]); keep(wv[1]); keep(wv[2]);
      STAMP(10 + 20 * L);
      f4 o[5];
      const v4u sv = ld16(c.ws, tok * 64 + g * 16, act + A_S2 + ttg * 1024);
      float rr = 0.f;
      gemm5<5, G5D>(c, lds, ga, wb, o, [&](int k) {
        if (k == -1) {
          float var;
          stats_from(sv, m2, var);
          r2 = __builtin_amdgcn_rsqf(var + LN_EPS);
          rr = r2 * __builtin_amdgcn_rsqf(var * r2 * r2 + LN_EPS);
          asm volatile("" : "+v"(m2), "+v"(r2), "+v"(rr));
        }
        if (k >= 7) W5(wa, OFF_W2, k + 5);
      });
#pragma unroll
      for (int e = 0; e < 12; ++e) W4(e);
      {
        const f2 mm = f2{-m2, -m2}, r2v = f2{rr, rr};
        f2 y[10];
#pragma unroll
        for (int nt = 0; nt < 5; ++nt) {
          const f4 cs = *(const LAS f4*)(lds + L_CS1 + (nt * 16 + 4 * g) * 4);
          y[2 * nt] = __builtin_elementwise_fma(mm, lo2(cs), lo2(o[nt])) * r2v;
          y[2 * nt + 1] = __builtin_elementwise_fma(mm, hi2(cs), hi2(o[nt])) * r2v;
        }
        gelu_n<10>(y, gk);
#pragma unroll
        for (int nt = 0; nt < 5; ++nt) o[nt] = f4{y[2 * nt][0], y[2 * nt][1], y[2 * nt + 1][0], y[2 * nt + 1][1]};
      }
      unsigned u[10];
      pack5(o, u);
      spread_loads<12, 10>();
      store5u(c, u, act + A_H, ttg, j);
      publish_n<0>(c, fgl + FG_H + p, j);
      { v4u t_[3]; warm5(c, act + A_H, ttg, j, t_); wv[0] = t_[0]; wv[1] = t_[1]; wv[2] = t_[2]; }
      STAMP(11 + 20 * L);
    }
    {
      PHASE_IDS();
      DMA_BASES();
      const int shift = w == 0 ? -1 : (w == 1 ? 1 : (w == 2 ? -2 : 2));
      const int pn = p + (shift < 0 ? -1 : 1);
      const bool nv = pn >= 0 && pn < 32;
      {
        const f2 mm = f2{-m2, -m2}, rv = f2{r2, r2};
#pragma unroll
        for (int nt = 0; nt < 5; ++nt) {
          const f2 a = (lo2(x2[nt]) + mm) * rv, b = (hi2(x2[nt]) + mm) * rv;
          x1[nt] = f4{a[0], a[1], b[0], b[1]};
        }
      }
      const G5 ga = gemm5_prep<5>(c, act + A_H, TILE6, p, shift);
      wait_flags(c, OFF_FLAG + (fgl + FG_H + ((lane & 2) && nv ? pn : p)) * 256 + (2 * w + (lane & 1)) * 8, lane < 4);
      keep(wv[0]); keep(wv[1]); keep(wv[2]);
      STAMP(12 + 20 * L);
      f4 o[5];
      gemm5<5, G5D>(c, lds, ga, wa, o, [&](int k) {
        if (k >= 8) W4(k + 4);
      });
#pragma unroll
      for (int e = 24; e < 36; ++e) W4(e);
#pragma unroll
      for (int nt = 0; nt < 5; ++nt) x1[nt] += o[nt];
      unsigned u[10];
      float rs, rq;
      pack5(x1, u);
      row_stats(x1, rs, rq);
      spread_loads<12, 6>();
      store5u(c, u, act + A_X1, ttg, j);
      store_sq(c, rs, rq, act + A_S1, ttg, j);
      publish_n<0>(c, fgl + FG_X1 + p, j);
      { v4u t_[3]; warm5(c, act + A_X1, ttg, j, t_); wv[0] = t_[0]; wv[1] = t_[1]; wv[2] = t_[2]; }
      STAMP(13 + 20 * L);
#pragma unroll
      for (int i = 24; i < 30; ++i) W3DMA(i);
    }
    {
      PHASE_IDS();
      DMA_BASES();
      const GeluK gk = gelu_consts();
      const int tt0 = 8 * q6 + 2 * w;
      const unsigned xv = lane * 16 + tt0 * TILE6;
      wait_group(c, fgl + FG_X1 + 2 * q6 + (w >> 1), 0, 8);
      keep(wv[0]); keep(wv[1]); keep(wv[2]);
      STAMP(14 + 20 * L);
      v4u sv[2];
#pragma unroll
      for (int t2 = 0; t2 < 2; ++t2) sv[t2] = ld16(c.ws, tok * 64 + g * 16, act + A_S1 + (tt0 + t2) * 1024);
      SB();
      float mean6[2], rstd6[2];
      f4 acc[6][2];
      gemm_lds<6, 2, P6LK>(c, lds + L_BIG + lane * 16, acc,
                           [&](int t, int t2) { return ld16(c.ws, xv + t2 * TILE6 + (t & 3) * 1024, act + A_X1 + (t >> 2) * 4096); },
                           [&](int t, int nt) {
                             constexpr int T0 = 21 - P6LK;
                             if (t < T0) { if (nt == 3 && (t & 1) == 0 && 36 + t / 2 < 44) W4(36 + t / 2); }
                             else {
                               const int e = 44 + 4 * (t - T0) + (nt - 2);
                               if (nt >= 2 && e < 60) W4(e);
                             }
                           },
                           [&]() {
#pragma unroll
                             for (int t2 = 0; t2 < 2; ++t2) {
                               float var;
                               stats_from(sv[t2], mean6[t2], var);
                               rstd6[t2] = __builtin_amdgcn_rsqf(var + LN_EPS);
                               asm volatile("" : "+v"(mean6[t2]), "+v"(rstd6[t2]));
                             }
                           });
#pragma unroll
      for (int e = 44 + 4 * (P6LK - 1); e < 60; ++e) W4(e);
      FSTAMP(44);
#pragma unroll
      for (int t2 = 0; t2 < 2; ++t2) {
        const f2 mm = f2{-mean6[t2], -mean6[t2]}, rv = f2{rstd6[t2], rstd6[t2]};
#pragma unroll
        for (int a = 0; a < 3; ++a) {
          f2 y[4];
#pragma unroll
          for (int hf = 0; hf < 2; ++hf) {
            const int nt = 2 * a + hf;
            const f4 cs = *(const LAS f4*)(lds + L_CS3 + (nt * 16 + 4 * g) * 4);
            y[2 * hf] = __builtin_elementwise_fma(mm, lo2(cs), lo2(acc[nt][t2])) * rv;
            y[2 * hf + 1] = __builtin_elementwise_fma(mm, hi2(cs), hi2(acc[nt][t2])) * rv;
          }
          gelu_n<4>(y, gk);
          st16(c.ws, g * 256 + tok * 16, act + A_H2 + (tt0 + t2) * TILEF + (12 * i6 + 4 * a) * 256,
               v4u{pk2(y[0][0], y[0][1]), pk2(y[1][0], y[1][1]), pk2(y[2][0], y[2][1]), pk2(y[3][0], y[3][1])});
          SB();
        }
      }
      SB();
      FSTAMP(45);
      publish(c, fgl + FG_H2 + q6 * 2 + (i6 >> 3), i6 & 7);
#pragma unroll
      for (int e = 0; e < 6; ++e) wv[e] = ld16(c.ws, lane * 16, act + A_H2 + (tt0 + e / 3) * TILEF + (12 * i6 + 4 * (e % 3)) * 256);
      STAMP(15 + 20 * L);
      if (L == 0) {
#pragma unroll
        for (int u = 0; u < 12; ++u) WQDMA(u);
      }
    }
    {
      PHASE_IDS();
      DMA_BASES();
#pragma unroll
      for (int nt = 0; nt < 5; ++nt) x2[nt] += x1[nt];
      const G5 ga = gemm5_prep<12>(c, act + A_H2, TILEF, p, 0);
      wait_group(c, fgl + FG_H2 + (p >> 1) * 2 + (w >> 1), 4 * (w & 1), 4);
      keep(wv[0]); keep(wv[1]); keep(wv[2]); keep(wv[3]); keep(wv[4]); keep(wv[5]);
      STAMP(16 + 20 * L);
      const unsigned tqv = L == 0 ? (unsigned)(lane * 16) : OOB;
      f4 o[5];
      gemm5<12, G5D + 2>(c, lds, ga, w4f, o, [&](int k) {
        if (k >= 38 && k < 46) dma16(c.ws, lds + wql + (k - 26) * 1024, tqv, wqg + (k - 26) * 1024);
      });
      FSTAMP(48);
#pragma unroll
      for (int nt = 0; nt < 5; ++nt) x2[nt] += o[nt];
      if (L == 0) {
#pragma unroll
        for (int i = 0; i < 12; ++i) W5(wa, OFF_WO, i);
        unsigned u[10];
        float rs, rq;
        pack5(x2, u);
        row_stats(x2, rs, rq);
        spread_loads<12, 5>();
        store5u(c, u, OFF_ACT + A_X3, ttg, j);
        store_sq(c, rs, rq, OFF_ACT + A_S3, ttg, j);
        publish_n<0>(c, fgl + FG_X3 + p, j);
        { v4u t_[3]; warm5(c, OFF_ACT + A_X3, ttg, j, t_); DRAIN(); keep(t_[0]); keep(t_[1]); keep(t_[2]); }
        STAMP(17 + 20 * L);
      } else {
        const rsrc_t ro = __builtin_amdgcn_make_buffer_rsrc((void*)P.out, 0, SEQ * DM * 2, 0x00020000);
        const unsigned vo = (unsigned)((16 * ttg + tok) * (DM * 2) + 160 * j);
        __builtin_amdgcn_raw_buffer_store_b128(v4u{pk2(x2[0][0], x2[0][1]), pk2(x2[0][2], x2[0][3]), pk2(x2[1][0], x2[1][1]), pk2(x2[1][2], x2[1][3])},
                                               ro, vo + g * 16, 0, 0);
        __builtin_amdgcn_raw_buffer_store_b128(v4u{pk2(x2[2][0], x2[2][1]), pk2(x2[2][2], x2[2][3]), pk2(x2[3][0], x2[3][1]), pk2(x2[3][2], x2[3][3])},
                                               ro, vo + 64 + g * 16, 0, 0);
        __builtin_amdgcn_raw_buffer_store_b64(v2u{pk2(x2[4][0], x2[4][1]), pk2(x2[4][2], x2[4][3])}, ro, vo + 128 + g * 8, 0, 0);
        STAMP(17 + 20 * L);
      }
    }
  }
  CSTAMP(63);
  SPAN(1);
  if (blockIdx.x == 0 && threadIdx.x == 0) __hip_atomic_store((GAS u64*)(P.ws + OFF_EP), c0.tag, __ATOMIC_RELAXED, __HIP_MEMORY_SCOPE_AGENT);
}

}

extern "C" void kernel_launch(void* const* d_in, const int* in_sizes, int n_in, void* d_out, int out_size, void* d_ws, size_t ws_size,
                              hipStream_t stream) {
  (void)in_sizes; (void)n_in; (void)out_size; (void)ws_size;
  hk::Params P;
  P.x = (const unsigned short*)d_in[0];
  P.wqkv = (const unsigned short*)d_in[1];
  P.wphi = (const unsigned short*)d_in[2];
  P.wo = (const unsigned short*)d_in[3];
  P.w1 = (const unsigned short*)d_in[5];
  P.w2 = (const unsigned short*)d_in[6];
  P.w3 = (const unsigned short*)d_in[7];
  P.w4 = (const unsigned short*)d_in[8];
  P.out = (unsigned short*)d_out;
  P.ws = (char*)d_ws;
  (void)hipFuncSetAttribute((const void*)hk::fused_kernel, hipFuncAttributeMaxDynamicSharedMemorySize, hk::LDS_BYTES);
  hk::fused_kernel<<<256, 256, hk::LDS_BYTES, stream>>>(P);
}
```
